```python
import torch

def _softmax(x, axis=-1):
    return torch.softmax(x, dim=axis)
def _gelu(x):
    return torch.nn.functional.gelu(x, approximate="tanh")
def _silu(x):
    return torch.nn.functional.silu(x)
def _sigmoid(x):
    return torch.sigmoid(x)
def _layernorm(x, axis=-1, eps=1e-5):
    m = x.mean(dim=axis, keepdim=True)
    v = x.var(dim=axis, keepdim=True, unbiased=False)
    return (x - m) / torch.sqrt(v + eps)
def _scatter_add(tgt, idx, src, axis=0):
    return tgt.clone().index_add_(axis, idx, src)
def _segment_sum(src, seg, n):
    out = torch.zeros((n,) + src.shape[1:], dtype=src.dtype)
    return out.index_add_(0, seg, src)
def _pad(x, pw):
    flat = tuple(v for pair in reversed(pw) for v in pair)
    return torch.nn.functional.pad(x, flat)

kernel_name = 'gather_scatter__cheap_equivariant_egnn__graph'

def setup_inputs():

    _w = (lambda a: (lambda t: t.to(torch.bfloat16) if t.is_floating_point() else t)(torch.as_tensor(a)))
    N, deg, D, De = 50000, 16, 64, max(0, 1)
    E = N * deg
    return {
        "h": _w((lambda *s: torch.randn(*s, dtype=torch.bfloat16) * 0.2)(N, D)),
        "pos": _w((lambda *s: torch.randn(*s, dtype=torch.bfloat16) * 0.4)(N, 3)),
        "e_attr": _w((lambda *s: torch.randn(*s, dtype=torch.bfloat16) * 0.4)(E, De)),
        "src": _w((lambda lo, hi, *s: torch.randint(lo, hi, s))(0, N, E).to(torch.int64)),
        "dst": _w((lambda lo, hi, *s: torch.randint(lo, hi, s))(0, N, E).to(torch.int64)),
        "w_e": _w(0.1 * (lambda *s: torch.randn(*s, dtype=torch.bfloat16) * 0.4)(2 * D + 1 + De, D)),
        "w_x": _w(0.1 * (lambda *s: torch.randn(*s, dtype=torch.bfloat16) * 0.2)(D, 1)),
        "w_h": _w(0.1 * (lambda *s: torch.randn(*s, dtype=torch.bfloat16) * 0.2)(2 * D, D)),
    }

def reference(h, pos, e_attr, src, dst, w_e, w_x, w_h):
    N, D = h.shape[0], h.shape[1]
    diff = pos[dst] - pos[src]
    dist2 = torch.sum(diff * diff, axis=-1, keepdims=True)
    e_in = torch.concatenate([h[dst], h[src], dist2, e_attr], axis=-1)
    m_ij = _silu(e_in @ w_e)
    coord_scale = m_ij @ w_x
    pos_new = pos + _segment_sum(diff * coord_scale, dst, N)
    m_i = _segment_sum(m_ij, dst, N)
    h_new = h + _silu(torch.concatenate([h, m_i], axis=-1) @ w_h)
    return h_new, pos_new
```

```cpp
#include <hip/hip_runtime.h>
#include <hip/hip_bf16.h>
#include <stdint.h>

namespace {

typedef uint16_t u16;
typedef uint32_t u32;
typedef uint64_t u64;
typedef __attribute__((ext_vector_type(2))) u32 u32x2;
typedef __attribute__((ext_vector_type(4))) u32 u32x4;
typedef __attribute__((ext_vector_type(4))) float f32x4;
typedef __attribute__((ext_vector_type(16))) float f32x16;
typedef __attribute__((ext_vector_type(8))) __bf16 bf16x8;
typedef __attribute__((ext_vector_type(2))) __bf16 bf16x2;

constexpr int NN = 50000;
constexpr int NE = 800000;
constexpr int R = 196;
constexpr int NB = 256;
constexpr int NP = 250;
constexpr int CH = 3200;
constexpr int CAP = 4096;
constexpr float LOG2E = 1.4426950408889634f;
constexpr int AUX_DEV = 16;
constexpr float LN2 = 0.6931471805599453f;

constexpr size_t WS_REC = 0;
constexpr size_t WS_OFF = WS_REC + (size_t)NP * CH * 8;
constexpr size_t WS_POS = WS_OFF + (size_t)256 * 256 * 4;
constexpr size_t WS_W1F = WS_POS + (size_t)NN * 16;
constexpr size_t WS_W2F = WS_W1F + 8192;
constexpr size_t WS_WHF = WS_W2F + 8192;
constexpr size_t WS_CR = WS_WHF + 16384;
constexpr size_t WS_WXF = WS_CR + 1024;
constexpr size_t WS_OH = WS_WXF + 128;
constexpr int NFRAG = 512 + 512 + 1024 + 64 + 8 + 64;

constexpr int L_LA = 0;
constexpr int L_LD = L_LA + (CAP + 256) * 4;
constexpr int L_PD = L_LD + CAP + 256;
constexpr int L_HIST = L_PD + 200 * 16;
constexpr int L_START = L_PD + 4096;
constexpr int L_PO = L_START + 896;
constexpr int L_CR = L_PO + 4096;
constexpr int L_WX = L_CR + 1024;
constexpr int L_OH = L_WX + 128;
constexpr int L_REC = L_OH + 1024;
constexpr int L_SP = L_REC + 8 * 1024;
constexpr int L_HIST2 = L_SP + 8 * 2304;
constexpr int L_CUR2 = L_HIST2 + 208 * 4;
constexpr int L_AN = L_CUR2 + 208 * 4;
constexpr int L_STG = L_AN + 200 * 128;
constexpr int L_W2S = L_STG + 65536;
constexpr int L_TOTAL = L_W2S + 8192;
typedef char lds_size_check[(L_TOTAL <= 163840) ? 1 : -1];
typedef char lds_align_check[(L_AN % 128 == 0 && L_STG % 128 == 0 && L_OH % 16 == 0 && L_SP % 16 == 0 && L_PO % 16 == 0 && L_CR % 16 == 0 && L_HIST % 16 == 0 && L_START % 16 == 0 && L_LA % 16 == 0) ? 1 : -1];

__device__ __forceinline__ float bf2f(u32 b) { return __builtin_bit_cast(float, b << 16); }
__device__ __forceinline__ u32 pack2(float a, float b) {
  bf16x2 o = {(__bf16)a, (__bf16)b};
  return __builtin_bit_cast(u32, o);
}
__device__ __forceinline__ u32 wave_incl_scan(u32 v) {
  v += (u32)__builtin_amdgcn_update_dpp(0, (int)v, 0x111, 0xf, 0xf, false);
  v += (u32)__builtin_amdgcn_update_dpp(0, (int)v, 0x112, 0xf, 0xf, false);
  v += (u32)__builtin_amdgcn_update_dpp(0, (int)v, 0x114, 0xf, 0xf, false);
  v += (u32)__builtin_amdgcn_update_dpp(0, (int)v, 0x118, 0xf, 0xf, false);
  v += (u32)__builtin_amdgcn_update_dpp(0, (int)v, 0x142, 0xa, 0xf, false);
  v += (u32)__builtin_amdgcn_update_dpp(0, (int)v, 0x143, 0xc, 0xf, false);
  return v;
}
__device__ __forceinline__ int phi(int t, int c) { return 32 * t + 16 * ((c >> 2) & 1) + (c & 3) + 4 * (c >> 3); }
__device__ __forceinline__ int rho_hh(int r) { return (r >> 2) & 1; }
__device__ __forceinline__ int rho_reg(int r) { return (r & 3) + 4 * (r >> 3); }

__global__ __launch_bounds__(1024) void egnn_k1(const int* __restrict__ src, const int* __restrict__ dst,
                                                const u16* __restrict__ ea, const u16* __restrict__ pos,
                                                const u16* __restrict__ we, const u16* __restrict__ wx,
                                                const u16* __restrict__ wh, char* __restrict__ ws
                                                ) {
  __shared__ __attribute__((aligned(16))) u32 hist[256];
  __shared__ __attribute__((aligned(16))) u32 base[256];
  __shared__ __attribute__((aligned(16))) u64 srt[CH];
  const int t = threadIdx.x, p = blockIdx.x, lane = t & 63;
  const bool isE = t < CH / 4;
  int4 d4 = {0, 0, 0, 0}, s4 = {0, 0, 0, 0};
  u32x2 e2 = {0, 0};
  asm volatile("" ::"s"(pos), "s"(src), "s"(dst), "s"(ea));
  const bool isP = t >= 824;
  u32 pxy = 0, pz = 0;
  if (isP) {
    const u16* pp = pos + (p * 200 + (t - 824)) * 3;
    __builtin_memcpy(&pxy, pp, 4);
    pz = pp[2];
  }
  if (isE) {
    d4 = ((const int4*)(dst + p * CH))[t];
  }
  if (t < 256) hist[t] = 0;
  const int id = p * 64 + (t - 960);
  const bool isW = t >= 960 && id < NFRAG;
  u32 wv[8] = {0, 0, 0, 0, 0, 0, 0, 0};
  u32 woff = 0;
  float wsc = 1.f;
  int oh = -1;
  if (isW) {
    const u16* bp = we;
    int off, s1 = 64, s2 = 256;
    if (id < 512) {
      const int l = id & 63, s = (id >> 6) & 3, tt = id >> 8, r = l & 31, hk = l >> 5;
      off = (16 * s + 8 * hk) * 64 + phi(tt, 16 * rho_hh(r) + rho_reg(r));
      woff = (u32)WS_W1F + id * 16;
    } else if (id < 1024) {
      const int q = id - 512, l = q & 63, s = (q >> 6) & 3, tt = q >> 8, c = l & 31, hk = l >> 5;
      off = (64 + 16 * s + 8 * hk) * 64 + phi(tt, c);
      wsc = -LOG2E;
      woff = (u32)WS_W2F + q * 16;
    } else if (id < 2048) {
      const int q = id - 1024, l = q & 63, s = (q >> 6) & 7, tt = q >> 9, r = l & 31, hk = l >> 5;
      const int reg = rho_reg(r), f = 16 * (2 * tt + (reg >> 3)) + 8 * rho_hh(r) + (reg & 7);
      bp = wh;
      off = (16 * s + 8 * hk) * 64 + f;
      wsc = s < 4 ? -LOG2E : 1.f;
      woff = (u32)WS_WHF + q * 16;
    } else if (id < 2112) {
      const int q = id - 2048, row = q >> 3, ch = q & 7;
      off = (128 + (row & 1)) * 64 + 32 * (ch >> 2) + 4 * (ch & 3);
      s1 = 1;
      s2 = 16;
      wsc = -LOG2E;
      oh = row >= 6 ? -1 : 8;
      woff = (u32)WS_CR + q * 16;
    } else if (id >= 2120) {
      const int q = id - 2120, row = q & 31;
      off = 0;
      oh = row < 8 ? row : 8;
      woff = (u32)WS_OH + q * 16;
    } else {
      const int q = id - 2112, hh = q & 1, sp = (q >> 1) & 1, tt = q >> 2;
      bp = wx;
      off = 32 * tt + 16 * hh + 8 * sp;
      s1 = 1;
      s2 = 4;
      woff = (u32)WS_WXF + q * 16;
    }
#pragma unroll
    for (int j = 0; j < 8; j++) wv[j] = bp[off + (j & 3) * s1 + (j >> 2) * s2];
  }
  __syncthreads();
  u32 bk[4], rk[4], lo[4], dl[4];
  if (isE) {
    s4 = ((const int4*)(src + p * CH))[t];
    e2 = ((const u32x2*)(ea + p * CH))[t];
    __builtin_amdgcn_sched_barrier(0);
    const int dd[4] = {d4.x, d4.y, d4.z, d4.w};
#pragma unroll
    for (int i = 0; i < 4; i++) {
      bk[i] = (u32)__umul24((u32)dd[i], 85599u) >> 24;
      rk[i] = atomicAdd(&hist[bk[i]], 1u);
    }
  }
  __syncthreads();
  {
    const u32x4 c = ((const u32x4*)hist)[lane];
    const u32 tot = c.x + c.y + c.z + c.w;
    const u32 ex = wave_incl_scan(tot) - tot;
    const u32x4 b = {ex, ex + c.x, ex + c.x + c.y, ex + c.x + c.y + c.z};
    ((u32x4*)base)[lane] = b;
  }
  if (isE) {
    asm volatile("" : "+v"(d4.x), "+v"(d4.y), "+v"(d4.z), "+v"(d4.w), "+v"(s4.x), "+v"(s4.y), "+v"(s4.z), "+v"(s4.w), "+v"(e2.x), "+v"(e2.y));
    const int dd[4] = {d4.x, d4.y, d4.z, d4.w};
    const int ss[4] = {s4.x, s4.y, s4.z, s4.w};
    const u32 ee[4] = {e2.x << 16, e2.x & 0xffff0000u, e2.y << 16, e2.y & 0xffff0000u};
#pragma unroll
    for (int i = 0; i < 4; i++) {
      dl[i] = (u32)dd[i] - bk[i] * R;
      lo[i] = (u32)ss[i] | ee[i];
    }
#pragma unroll
    for (int i = 0; i < 4; i++) {
      const u32 q = base[bk[i]] + rk[i];
      srt[q] = (u64)lo[i] | ((u64)(4 * dl[i]) << 32);
    }
  }
  __builtin_amdgcn_s_waitcnt(0x0F70);
  {
    if (t < 256) ((u32*)(ws + WS_OFF))[t * 256 + (p & 7) * 32 + (p >> 3)] = base[t] | (hist[t] << 16);
    else if (p < 256 - NP && t < 512) ((u32*)(ws + WS_OFF))[(t - 256) * 256 + ((NP + p) & 7) * 32 + 31] = 0;
  }
  __syncthreads();
  const __amdgpu_buffer_rsrc_t srdW = __builtin_amdgcn_make_buffer_rsrc((void*)ws, 0, 1 << 24, 0x00020000);
  for (int i = t; i < CH / 2; i += 1024)
    __builtin_amdgcn_raw_buffer_store_b128(((const u32x4*)srt)[i], srdW, (int)WS_REC + (p * (CH / 2) + i) * 16, 0, AUX_DEV);
  asm volatile("" : "+v"(wv[0]), "+v"(wv[1]), "+v"(wv[2]), "+v"(wv[3]), "+v"(wv[4]), "+v"(wv[5]), "+v"(wv[6]), "+v"(wv[7]), "+v"(pxy), "+v"(pz));
  if (isW) {
    float v[8];
#pragma unroll
    for (int j = 0; j < 8; j++) v[j] = oh < 0 ? wsc * bf2f(wv[j]) : (oh == j ? 1.f : 0.f);
    u32x4 o = {pack2(v[0], v[1]), pack2(v[2], v[3]), pack2(v[4], v[5]), pack2(v[6], v[7])};
    __builtin_amdgcn_raw_buffer_store_b128(o, srdW, (int)woff, 0, AUX_DEV);
  }
  if (isP) {
    f32x4 v = {bf2f(pxy & 0xffffu), __builtin_bit_cast(float, pxy & 0xffff0000u), bf2f(pz), 0.f};
    __builtin_amdgcn_raw_buffer_store_b128(__builtin_bit_cast(u32x4, v), srdW, (int)WS_POS + (p * 200 + (t - 824)) * 16, 0, AUX_DEV);
  }
}

__device__ __forceinline__ float silu_true(float x) { return x * __builtin_amdgcn_rcpf(1.f + __builtin_amdgcn_exp2f(-LOG2E * x)); }

__device__ __forceinline__ void slow_edge(const u16* __restrict__ h, const u16* wl, const u16* __restrict__ wx, int lane,
                                          int sn, int dn, float d2, float eav, float& m, float& cs) {
  const float hs = bf2f(h[sn * 64 + lane]), hd = bf2f(h[dn * 64 + lane]);
  float x = d2 * bf2f(wl[128 * 64 + lane]) + eav * bf2f(wl[129 * 64 + lane]);
  for (int k = 0; k < 64; k++) {
    x = fmaf(__shfl(hd, k), bf2f(wl[k * 64 + lane]), x);
    x = fmaf(__shfl(hs, k), bf2f(wl[(64 + k) * 64 + lane]), x);
  }
  m = (float)(__bf16)silu_true(x);
  cs = m * bf2f(wx[lane]);
  for (int o = 32; o > 0; o >>= 1) cs += __shfl_xor(cs, o);
}

__global__ __launch_bounds__(512) void egnn_k2(const u16* __restrict__ h, const u16* __restrict__ we,
                                               const u16* __restrict__ wx, const char* __restrict__ ws,
                                               u16* __restrict__ out) {
  __shared__ __attribute__((aligned(128))) char smem[L_TOTAL];
  const int w = __builtin_amdgcn_readfirstlane(threadIdx.x >> 6);
  const int b = (blockIdx.x & 7) * 32 + (blockIdx.x >> 3);
  const int n0 = b * R;
  const int Rb = min(R, NN - n0);
  u32 flag;
  {
    const uint64_t hp = (uint64_t)h, wp = (uint64_t)ws, op = (uint64_t)out; (void)op;
    asm volatile(
      "s_mov_b32 s82, m0\n"
      "s_mov_b32 s64, %[hlo]\n"
      "s_and_b32 s65, %[hhi], 0xffff\n"
      "s_mov_b32 s66, 6400000\n"
      "s_mov_b32 s67, 0x20000\n"
      "s_mov_b32 s52, %[wlo]\n"
      "s_and_b32 s53, %[whi], 0xffff\n"
      "s_mov_b32 s54, 0x1000000\n"
      "s_mov_b32 s55, 0x20000\n"
      "s_add_u32 s68, %[wlo], 6662144\n"
      "s_addc_u32 s69, %[whi], 0\n"
      "s_and_b32 s69, s69, 0xffff\n"
      "s_mov_b32 s70, 800000\n"
      "s_mov_b32 s71, 0x20000\n"
      "v_mbcnt_lo_u32_b32 v208, -1, 0\n"
      "v_mbcnt_hi_u32_b32 v208, -1, v208\n"
      "v_and_b32 v209, 31, v208\n"
      "v_lshrrev_b32 v210, 5, v208\n"
      "v_lshlrev_b32 v211, 4, v208\n"
      "s_lshl_b32 s83, %[w], 7\n"
      "s_lshl_b32 s84, %[b], 10\n"
      "s_add_u32 s83, s83, s84\n"
      "s_add_u32 s83, s83, 6400000\n"
      "v_lshlrev_b32 v80, 2, v210\n"
      "buffer_load_dword v0, v80, s[52:55], s83 offen offset:0\n"
      "buffer_load_dword v1, v80, s[52:55], s83 offen offset:8\n"
      "buffer_load_dword v2, v80, s[52:55], s83 offen offset:16\n"
      "buffer_load_dword v3, v80, s[52:55], s83 offen offset:24\n"
      "buffer_load_dword v4, v80, s[52:55], s83 offen offset:32\n"
      "buffer_load_dword v5, v80, s[52:55], s83 offen offset:40\n"
      "buffer_load_dword v6, v80, s[52:55], s83 offen offset:48\n"
      "buffer_load_dword v7, v80, s[52:55], s83 offen offset:56\n"
      "buffer_load_dword v8, v80, s[52:55], s83 offen offset:64\n"
      "buffer_load_dword v9, v80, s[52:55], s83 offen offset:72\n"
      "buffer_load_dword v10, v80, s[52:55], s83 offen offset:80\n"
      "buffer_load_dword v11, v80, s[52:55], s83 offen offset:88\n"
      "buffer_load_dword v12, v80, s[52:55], s83 offen offset:96\n"
      "buffer_load_dword v13, v80, s[52:55], s83 offen offset:104\n"
      "buffer_load_dword v14, v80, s[52:55], s83 offen offset:112\n"
      "buffer_load_dword v15, v80, s[52:55], s83 offen offset:120\n"
      "s_mul_i32 s95, %[b], 196\n"
      "v_mov_b32 v88, 0\n"
      "v_mov_b32 v89, 0\n"
      "v_mov_b32 v90, 0\n"
      "v_mov_b32 v91, 0\n"
      "s_lshl_b32 s83, %[w], 9\n"
      "s_add_u32 s83, s83, 21760\n"
      "v_lshl_add_u32 v82, v208, 3, s83\n"
      "ds_write_b64 v82, v[88:89]\n"
      "s_mul_i32 s83, %[w], 2304\n"
      "s_add_u32 s83, s83, 41216\n"
      "v_add_u32 v82, s83, v211\n"
      "ds_write_b128 v82, v[88:91]\n"
      "ds_write_b128 v82, v[88:91] offset:1024\n"
      "s_mov_b64 exec, 0xffff\n"
      "ds_write_b128 v82, v[88:91] offset:2048\n"
      "s_mov_b64 exec, -1\n"
      "s_mul_i32 s83, %[w], 25600\n"
      "v_mul_u32_u24 v83, 204800, v210\n"
      "v_add_u32 v83, s83, v83\n"
      "v_mov_b32 v241, v211\n"
      "s_lshl_b32 s83, %[w], 12\n"
      "s_add_u32 s83, s83, 61312\n"
      "v_lshlrev_b32 v242, 7, v209\n"
      "v_lshl_add_u32 v242, v210, 5, v242\n"
      "v_add_u32 v242, s83, v242\n"
      "s_lshl_b32 s83, %[w], 5\n"
      "s_add_u32 s83, s83, s95\n"
      "s_lshl_b32 s83, s83, 7\n"
      "v_lshlrev_b32 v81, 7, v209\n"
      "v_lshl_add_u32 v81, v210, 4, v81\n"
      "v_add_u32 v81, s83, v81\n"
      "s_lshl_b32 s79, %[w], 13\n"
      "s_add_u32 s79, s79, 86912\n"
      "s_mov_b32 s80, 128\n"
      "s_mov_b32 s86, 0xffff\n"
      "s_mov_b32 s81, 0\n"
      "s_mov_b32 s85, 26752\n"
      "s_mov_b32 s87, 32000\n"
      "s_mov_b32 s94, 31872\n"
      "s_mov_b32 s76, 0\n"
      "v_and_b32 v84, 7, v208\n"
      "v_lshrrev_b32 v85, 4, v208\n"
      "v_xor_b32 v84, v84, v85\n"
      "v_lshlrev_b32 v226, 4, v84\n"
      "v_xor_b32 v227, 64, v226\n"
      "v_lshrrev_b32 v84, 1, v209\n"
      "v_and_b32 v84, 7, v84\n"
      "v_xor_b32 v84, v84, v210\n"
      "v_lshlrev_b32 v84, 4, v84\n"
      "v_lshl_add_u32 v84, v209, 7, v84\n"
      "v_add_u32 v222, s79, v84\n"
      "v_xor_b32 v223, 32, v222\n"
      "v_xor_b32 v224, 64, v222\n"
      "v_xor_b32 v225, 0x60, v222\n"
      "s_lshl_b32 s83, %[w], 10\n"
      "s_add_u32 s83, s83, 33024\n"
      "v_lshl_add_u32 v231, v209, 4, s83\n"
      "v_lshl_add_u32 v238, v208, 4, s83\n"
      "v_lshlrev_b32 v235, 3, v210\n"
      "v_lshlrev_b32 v84, 4, v210\n"
      "v_sub_u32 v234, 16, v84\n"
      "v_cmp_eq_u32 vcc, 0, v210\n"
      "v_mov_b32 v84, s87\n"
      "s_nop 0\n"
      "v_cndmask_b32 v232, v231, v84, vcc\n"
      "v_lshrrev_b32 v84, 2, v208\n"
      "v_and_b32 v84, 3, v84\n"
      "v_and_b32 v85, 3, v208\n"
      "v_lshlrev_b32 v85, 3, v85\n"
      "v_and_b32 v86, 16, v208\n"
      "v_lshl_add_u32 v85, v86, 1, v85\n"
      "v_lshl_add_u32 v86, v84, 7, v85\n"
      "v_mov_b32 v233, 30848\n"
      "v_mov_b32 v220, 61312\n"
      "v_cndmask_b32 v233, v233, v220, vcc\n"
      "v_add_u32 v233, v233, v86\n"
      "s_mul_i32 s83, %[w], 2304\n"
      "s_add_u32 s83, s83, 41216\n"
      "v_lshl_add_u32 v84, v210, 2, v84\n"
      "v_mul_u32_u24 v84, 72, v84\n"
      "v_add_u32 v84, v84, v85\n"
      "v_add_u32 v237, s83, v84\n"
      "v_mul_u32_u24 v84, 72, v209\n"
      "v_add_u32 v84, s83, v84\n"
      "v_lshlrev_b32 v85, 6, v210\n"
      "v_sub_u32 v85, 64, v85\n"
      "v_add_u32 v236, v84, v85\n"
      "v_lshrrev_b32 v84, 2, v209\n"
      "v_lshlrev_b32 v84, 7, v84\n"
      "v_lshl_add_u32 v84, v210, 5, v84\n"
      "v_add_u32 v239, 61312, v84\n"
      "v_lshlrev_b32 v84, 2, v208\n"
      "v_add_u32 v240, 21760, v84\n"
      "v_mov_b32 v220, 0\n"
      "v_mov_b32 v221, 0\n"
      "s_waitcnt vmcnt(15)\n"
      "v_add_u32_sdwa v48, v0, v209 dst_sel:DWORD dst_unused:UNUSED_PAD src0_sel:WORD_0 src1_sel:DWORD\n"
      "v_cmp_gt_u32_sdwa s[20:21], v0, v209 src0_sel:WORD_1 src1_sel:DWORD\n"
      "v_lshl_add_u32 v48, v48, 3, v83\n"
      "s_waitcnt vmcnt(14)\n"
      "v_add_u32_sdwa v49, v1, v209 dst_sel:DWORD dst_unused:UNUSED_PAD src0_sel:WORD_0 src1_sel:DWORD\n"
      "v_cmp_gt_u32_sdwa s[22:23], v1, v209 src0_sel:WORD_1 src1_sel:DWORD\n"
      "v_lshl_add_u32 v49, v49, 3, v83\n"
      "s_waitcnt vmcnt(13)\n"
      "v_add_u32_sdwa v50, v2, v209 dst_sel:DWORD dst_unused:UNUSED_PAD src0_sel:WORD_0 src1_sel:DWORD\n"
      "v_cmp_gt_u32_sdwa s[24:25], v2, v209 src0_sel:WORD_1 src1_sel:DWORD\n"
      "v_lshl_add_u32 v50, v50, 3, v83\n"
      "s_waitcnt vmcnt(12)\n"
      "v_add_u32_sdwa v51, v3, v209 dst_sel:DWORD dst_unused:UNUSED_PAD src0_sel:WORD_0 src1_sel:DWORD\n"
      "v_cmp_gt_u32_sdwa s[26:27], v3, v209 src0_sel:WORD_1 src1_sel:DWORD\n"
      "v_lshl_add_u32 v51, v51, 3, v83\n"
      "s_mov_b64 exec, s[20:21]\n"
      "s_mov_b32 s83, 0\n"
      "buffer_load_dwordx2 v[16:17], v48, s[52:55], s83 offen\n"
      "s_mov_b64 exec, s[22:23]\n"
      "s_mov_b32 s83, 409600\n"
      "buffer_load_dwordx2 v[18:19], v49, s[52:55], s83 offen\n"
      "s_mov_b64 exec, s[24:25]\n"
      "s_mov_b32 s83, 819200\n"
      "buffer_load_dwordx2 v[20:21], v50, s[52:55], s83 offen\n"
      "s_mov_b64 exec, s[26:27]\n"
      "s_mov_b32 s83, 1228800\n"
      "buffer_load_dwordx2 v[22:23], v51, s[52:55], s83 offen\n"
      "s_mov_b64 exec, -1\n"
      "s_waitcnt vmcnt(15)\n"
      "v_add_u32_sdwa v52, v4, v209 dst_sel:DWORD dst_unused:UNUSED_PAD src0_sel:WORD_0 src1_sel:DWORD\n"
      "v_cmp_gt_u32_sdwa s[28:29], v4, v209 src0_sel:WORD_1 src1_sel:DWORD\n"
      "v_lshl_add_u32 v52, v52, 3, v83\n"
      "s_waitcnt vmcnt(14)\n"
      "v_add_u32_sdwa v53, v5, v209 dst_sel:DWORD dst_unused:UNUSED_PAD src0_sel:WORD_0 src1_sel:DWORD\n"
      "v_cmp_gt_u32_sdwa s[30:31], v5, v209 src0_sel:WORD_1 src1_sel:DWORD\n"
      "v_lshl_add_u32 v53, v53, 3, v83\n"
      "s_waitcnt vmcnt(13)\n"
      "v_add_u32_sdwa v54, v6, v209 dst_sel:DWORD dst_unused:UNUSED_PAD src0_sel:WORD_0 src1_sel:DWORD\n"
      "v_cmp_gt_u32_sdwa s[34:35], v6, v209 src0_sel:WORD_1 src1_sel:DWORD\n"
      "v_lshl_add_u32 v54, v54, 3, v83\n"
      "s_waitcnt vmcnt(12)\n"
      "v_add_u32_sdwa v55, v7, v209 dst_sel:DWORD dst_unused:UNUSED_PAD src0_sel:WORD_0 src1_sel:DWORD\n"
      "v_cmp_gt_u32_sdwa s[36:37], v7, v209 src0_sel:WORD_1 src1_sel:DWORD\n"
      "v_lshl_add_u32 v55, v55, 3, v83\n"
      "s_mov_b64 exec, s[28:29]\n"
      "s_mov_b32 s83, 1638400\n"
      "buffer_load_dwordx2 v[24:25], v52, s[52:55], s83 offen\n"
      "s_mov_b64 exec, s[30:31]\n"
      "s_mov_b32 s83, 2048000\n"
      "buffer_load_dwordx2 v[26:27], v53, s[52:55], s83 offen\n"
      "s_mov_b64 exec, s[34:35]\n"
      "s_mov_b32 s83, 2457600\n"
      "buffer_load_dwordx2 v[28:29], v54, s[52:55], s83 offen\n"
      "s_mov_b64 exec, s[36:37]\n"
      "s_mov_b32 s83, 2867200\n"
      "buffer_load_dwordx2 v[30:31], v55, s[52:55], s83 offen\n"
      "s_mov_b64 exec, -1\n"
      "s_waitcnt vmcnt(15)\n"
      "v_add_u32_sdwa v56, v8, v209 dst_sel:DWORD dst_unused:UNUSED_PAD src0_sel:WORD_0 src1_sel:DWORD\n"
      "v_cmp_gt_u32_sdwa s[38:39], v8, v209 src0_sel:WORD_1 src1_sel:DWORD\n"
      "v_lshl_add_u32 v56, v56, 3, v83\n"
      "s_waitcnt vmcnt(14)\n"
      "v_add_u32_sdwa v57, v9, v209 dst_sel:DWORD dst_unused:UNUSED_PAD src0_sel:WORD_0 src1_sel:DWORD\n"
      "v_cmp_gt_u32_sdwa s[40:41], v9, v209 src0_sel:WORD_1 src1_sel:DWORD\n"
      "v_lshl_add_u32 v57, v57, 3, v83\n"
      "s_waitcnt vmcnt(13)\n"
      "v_add_u32_sdwa v58, v10, v209 dst_sel:DWORD dst_unused:UNUSED_PAD src0_sel:WORD_0 src1_sel:DWORD\n"
      "v_cmp_gt_u32_sdwa s[42:43], v10, v209 src0_sel:WORD_1 src1_sel:DWORD\n"
      "v_lshl_add_u32 v58, v58, 3, v83\n"
      "s_waitcnt vmcnt(12)\n"
      "v_add_u32_sdwa v59, v11, v209 dst_sel:DWORD dst_unused:UNUSED_PAD src0_sel:WORD_0 src1_sel:DWORD\n"
      "v_cmp_gt_u32_sdwa s[44:45], v11, v209 src0_sel:WORD_1 src1_sel:DWORD\n"
      "v_lshl_add_u32 v59, v59, 3, v83\n"
      "s_mov_b64 exec, s[38:39]\n"
      "s_mov_b32 s83, 3276800\n"
      "buffer_load_dwordx2 v[32:33], v56, s[52:55], s83 offen\n"
      "s_mov_b64 exec, s[40:41]\n"
      "s_mov_b32 s83, 3686400\n"
      "buffer_load_dwordx2 v[34:35], v57, s[52:55], s83 offen\n"
      "s_mov_b64 exec, s[42:43]\n"
      "s_mov_b32 s83, 4096000\n"
      "buffer_load_dwordx2 v[36:37], v58, s[52:55], s83 offen\n"
      "s_mov_b64 exec, s[44:45]\n"
      "s_mov_b32 s83, 4505600\n"
      "buffer_load_dwordx2 v[38:39], v59, s[52:55], s83 offen\n"
      "s_mov_b64 exec, -1\n"
      "s_waitcnt vmcnt(15)\n"
      "v_add_u32_sdwa v60, v12, v209 dst_sel:DWORD dst_unused:UNUSED_PAD src0_sel:WORD_0 src1_sel:DWORD\n"
      "v_cmp_gt_u32_sdwa s[46:47], v12, v209 src0_sel:WORD_1 src1_sel:DWORD\n"
      "v_lshl_add_u32 v60, v60, 3, v83\n"
      "s_waitcnt vmcnt(14)\n"
      "v_add_u32_sdwa v61, v13, v209 dst_sel:DWORD dst_unused:UNUSED_PAD src0_sel:WORD_0 src1_sel:DWORD\n"
      "v_cmp_gt_u32_sdwa s[48:49], v13, v209 src0_sel:WORD_1 src1_sel:DWORD\n"
      "v_lshl_add_u32 v61, v61, 3, v83\n"
      "s_waitcnt vmcnt(13)\n"
      "v_add_u32_sdwa v62, v14, v209 dst_sel:DWORD dst_unused:UNUSED_PAD src0_sel:WORD_0 src1_sel:DWORD\n"
      "v_cmp_gt_u32_sdwa s[50:51], v14, v209 src0_sel:WORD_1 src1_sel:DWORD\n"
      "v_lshl_add_u32 v62, v62, 3, v83\n"
      "s_waitcnt vmcnt(12)\n"
      "v_add_u32_sdwa v63, v15, v209 dst_sel:DWORD dst_unused:UNUSED_PAD src0_sel:WORD_0 src1_sel:DWORD\n"
      "v_cmp_gt_u32_sdwa s[92:93], v15, v209 src0_sel:WORD_1 src1_sel:DWORD\n"
      "v_lshl_add_u32 v63, v63, 3, v83\n"
      "s_mov_b64 exec, s[46:47]\n"
      "s_mov_b32 s83, 4915200\n"
      "buffer_load_dwordx2 v[40:41], v60, s[52:55], s83 offen\n"
      "s_mov_b64 exec, s[48:49]\n"
      "s_mov_b32 s83, 5324800\n"
      "buffer_load_dwordx2 v[42:43], v61, s[52:55], s83 offen\n"
      "s_mov_b64 exec, s[50:51]\n"
      "s_mov_b32 s83, 5734400\n"
      "buffer_load_dwordx2 v[44:45], v62, s[52:55], s83 offen\n"
      "s_mov_b64 exec, s[92:93]\n"
      "s_mov_b32 s83, 6144000\n"
      "buffer_load_dwordx2 v[46:47], v63, s[52:55], s83 offen\n"
      "s_mov_b64 exec, -1\n"
      "v_max3_u32 v80, v0, v1, v2\n"
      "v_max3_u32 v80, v80, v3, v4\n"
      "v_max3_u32 v80, v80, v5, v6\n"
      "v_max3_u32 v80, v80, v7, v8\n"
      "v_max3_u32 v80, v80, v9, v10\n"
      "v_max3_u32 v80, v80, v11, v12\n"
      "v_max3_u32 v80, v80, v13, v14\n"
      "v_max_u32 v80, v80, v15\n"
      "v_mov_b32 v86, v81\n"
      "v_cmp_lt_u32 vcc, 0x20ffff, v80\n"
      "s_cmp_lg_u64 vcc, 0\n"
      "s_cselect_b32 s95, 0x1000000, 0\n"
      "v_mov_b32 v81, s95\n"
      "v_mov_b32 v82, 0\n"
      "v_mov_b32 v80, 1\n"
      "s_lshl_b32 s83, %[w], 5\n"
      "v_add_u32 v83, s83, v209\n"
      "v_mov_b32 v84, 200\n"
      "v_cmp_gt_u32_e64 s[56:57], v84, v83\n"
      "v_mov_b32 v96, 0\n"
      "v_mov_b32 v97, 0\n"
      "v_mov_b32 v98, 0\n"
      "v_mov_b32 v99, 0\n"
      "v_mov_b32 v100, 0\n"
      "v_mov_b32 v101, 0\n"
      "v_mov_b32 v102, 0\n"
      "v_mov_b32 v103, 0\n"
      "v_mov_b32 v104, 0\n"
      "v_mov_b32 v105, 0\n"
      "v_mov_b32 v106, 0\n"
      "v_mov_b32 v107, 0\n"
      "v_mov_b32 v108, 0\n"
      "v_mov_b32 v109, 0\n"
      "v_mov_b32 v110, 0\n"
      "v_mov_b32 v111, 0\n"
      "v_mov_b32 v112, 0\n"
      "v_mov_b32 v113, 0\n"
      "v_mov_b32 v114, 0\n"
      "v_mov_b32 v115, 0\n"
      "v_mov_b32 v116, 0\n"
      "v_mov_b32 v117, 0\n"
      "v_mov_b32 v118, 0\n"
      "v_mov_b32 v119, 0\n"
      "v_mov_b32 v120, 0\n"
      "v_mov_b32 v121, 0\n"
      "v_mov_b32 v122, 0\n"
      "v_mov_b32 v123, 0\n"
      "v_mov_b32 v124, 0\n"
      "v_mov_b32 v125, 0\n"
      "v_mov_b32 v126, 0\n"
      "v_mov_b32 v127, 0\n"
      "v_mov_b32 v128, 0\n"
      "v_mov_b32 v129, 0\n"
      "v_mov_b32 v130, 0\n"
      "v_mov_b32 v131, 0\n"
      "v_mov_b32 v132, 0\n"
      "v_mov_b32 v133, 0\n"
      "v_mov_b32 v134, 0\n"
      "v_mov_b32 v135, 0\n"
      "v_mov_b32 v136, 0\n"
      "v_mov_b32 v137, 0\n"
      "v_mov_b32 v138, 0\n"
      "v_mov_b32 v139, 0\n"
      "v_mov_b32 v140, 0\n"
      "v_mov_b32 v141, 0\n"
      "v_mov_b32 v142, 0\n"
      "v_mov_b32 v143, 0\n"
      "v_mov_b32 v176, 0\n"
      "v_mov_b32 v177, 0\n"
      "v_mov_b32 v178, 0\n"
      "v_mov_b32 v179, 0\n"
      "s_waitcnt lgkmcnt(0)\n"
      "s_barrier\n"
      "s_lshl_b32 s84, %[w], 10\n"
      "s_add_u32 s83, s84, 7462144\n"
      "s_add_u32 m0, s84, 33024\n"
      "s_nop 0\n"
      "buffer_load_dwordx4 v211, s[52:55], s83 offen lds\n"
      "s_add_u32 s83, s83, 8192\n"
      "s_add_u32 m0, s84, 152448\n"
      "s_nop 0\n"
      "buffer_load_dwordx4 v211, s[52:55], s83 offen lds\n"
      "s_mul_i32 s95, %[b], 196\n"
      "s_lshl_b32 s83, s95, 4\n"
      "s_min_u32 s84, %[w], 6\n"
      "s_lshl_b32 s84, s84, 10\n"
      "s_cmp_lt_u32 %[w], 4\n"
      "s_cselect_b64 s[56:57], s[68:69], s[52:53]\n"
      "s_cselect_b32 s58, s70, s54\n"
      "s_mov_b32 s59, 0x20000\n"
      "s_cselect_b32 s83, s83, 7490816\n"
      "s_add_u32 s83, s83, s84\n"
      "s_add_u32 m0, s84, 26752\n"
      "s_cmp_eq_u32 %[w], 6\n"
      "s_cselect_b64 exec, 0xff, -1\n"
      "s_cmp_eq_u32 %[w], 7\n"
      "s_cselect_b64 exec, 0xff, exec\n"
      "buffer_load_dwordx4 v211, s[56:59], s83 offen lds\n"
      "s_mov_b64 exec, -1\n"
      "buffer_load_dwordx4 v[0:3], v86, s[64:67], 0 offen offset:0\n"
      "buffer_load_dwordx4 v[4:7], v86, s[64:67], 0 offen offset:32\n"
      "buffer_load_dwordx4 v[8:11], v86, s[64:67], 0 offen offset:64\n"
      "buffer_load_dwordx4 v[12:15], v86, s[64:67], 0 offen offset:96\n"
      "v_cmp_gt_u32_e64 s[56:57], v84, v83\n"
      "s_mov_b64 exec, s[20:21]\n"
      "s_waitcnt vmcnt(22)\n"
      "ds_add_rtn_u32 v48, v17, v80 offset:24960\n"
      "s_mov_b64 exec, s[22:23]\n"
      "s_waitcnt vmcnt(21)\n"
      "ds_add_rtn_u32 v49, v19, v80 offset:24960\n"
      "s_mov_b64 exec, s[24:25]\n"
      "s_waitcnt vmcnt(20)\n"
      "ds_add_rtn_u32 v50, v21, v80 offset:24960\n"
      "s_mov_b64 exec, s[26:27]\n"
      "s_waitcnt vmcnt(19)\n"
      "ds_add_rtn_u32 v51, v23, v80 offset:24960\n"
      "s_mov_b64 exec, s[28:29]\n"
      "s_waitcnt vmcnt(18)\n"
      "ds_add_rtn_u32 v52, v25, v80 offset:24960\n"
      "s_mov_b64 exec, s[30:31]\n"
      "s_waitcnt vmcnt(17)\n"
      "ds_add_rtn_u32 v53, v27, v80 offset:24960\n"
      "s_mov_b64 exec, s[34:35]\n"
      "s_waitcnt vmcnt(16)\n"
      "ds_add_rtn_u32 v54, v29, v80 offset:24960\n"
      "s_mov_b64 exec, s[36:37]\n"
      "s_waitcnt vmcnt(15)\n"
      "ds_add_rtn_u32 v55, v31, v80 offset:24960\n"
      "s_mov_b64 exec, s[38:39]\n"
      "s_waitcnt vmcnt(14)\n"
      "ds_add_rtn_u32 v56, v33, v80 offset:24960\n"
      "s_mov_b64 exec, s[40:41]\n"
      "s_waitcnt vmcnt(13)\n"
      "ds_add_rtn_u32 v57, v35, v80 offset:24960\n"
      "s_mov_b64 exec, s[42:43]\n"
      "s_waitcnt vmcnt(12)\n"
      "ds_add_rtn_u32 v58, v37, v80 offset:24960\n"
      "s_mov_b64 exec, s[44:45]\n"
      "s_waitcnt vmcnt(11)\n"
      "ds_add_rtn_u32 v59, v39, v80 offset:24960\n"
      "s_mov_b64 exec, s[46:47]\n"
      "s_waitcnt vmcnt(10)\n"
      "ds_add_rtn_u32 v60, v41, v80 offset:24960\n"
      "s_mov_b64 exec, s[48:49]\n"
      "s_waitcnt vmcnt(9)\n"
      "ds_add_rtn_u32 v61, v43, v80 offset:24960\n"
      "s_mov_b64 exec, s[50:51]\n"
      "s_waitcnt vmcnt(8)\n"
      "ds_add_rtn_u32 v62, v45, v80 offset:24960\n"
      "s_mov_b64 exec, s[92:93]\n"
      "s_waitcnt vmcnt(7)\n"
      "ds_add_rtn_u32 v63, v47, v80 offset:24960\n"
      "s_mov_b64 exec, 1\n"
      "ds_add_u32 v82, v81 offset:24960\n"
      "s_mov_b64 exec, -1\n"
      "s_waitcnt lgkmcnt(0)\n"
      "s_barrier\n"
      "s_bfm_b64 exec, 52, 0\n"
      "ds_read_b128 v[176:179], v211 offset:24960\n"
      "s_mov_b64 exec, -1\n"
      "s_waitcnt lgkmcnt(0)\n"
      "v_add3_u32 v80, v176, v177, v178\n"
      "v_add_u32 v80, v80, v179\n"
      "v_mov_b32 v81, v80\n"
      "s_nop 1\n"
      "v_add_u32_dpp v81, v81, v81 row_shr:1 row_mask:0xf bank_mask:0xf bound_ctrl:1\n"
      "s_nop 1\n"
      "v_add_u32_dpp v81, v81, v81 row_shr:2 row_mask:0xf bank_mask:0xf bound_ctrl:1\n"
      "s_nop 1\n"
      "v_add_u32_dpp v81, v81, v81 row_shr:4 row_mask:0xf bank_mask:0xf bound_ctrl:1\n"
      "s_nop 1\n"
      "v_add_u32_dpp v81, v81, v81 row_shr:8 row_mask:0xf bank_mask:0xf bound_ctrl:1\n"
      "s_nop 1\n"
      "v_add_u32_dpp v81, v81, v81 row_bcast:15 row_mask:0xa bank_mask:0xf\n"
      "s_nop 1\n"
      "v_add_u32_dpp v81, v81, v81 row_bcast:31 row_mask:0xc bank_mask:0xf\n"
      "v_sub_u32 v180, v81, v80\n"
      "v_readlane_b32 s60, v81, 63\n"
      "v_add_u32 v181, v180, v176\n"
      "v_add_u32 v182, v181, v177\n"
      "v_add_u32 v183, v182, v178\n"
      "s_bfm_b64 exec, 52, 0\n"
      "ds_write_b128 v211, v[180:183] offset:25856\n"
      "s_cmp_gt_u32 s60, 4096\n"
      "s_cbranch_scc1 LBAIL_%=\n"
      "s_mov_b64 exec, s[20:21]\n"
      "ds_read_b32 v64, v17 offset:25856\n"
      "s_mov_b64 exec, s[22:23]\n"
      "ds_read_b32 v65, v19 offset:25856\n"
      "s_mov_b64 exec, s[24:25]\n"
      "ds_read_b32 v66, v21 offset:25856\n"
      "s_mov_b64 exec, s[26:27]\n"
      "ds_read_b32 v67, v23 offset:25856\n"
      "s_mov_b64 exec, s[28:29]\n"
      "ds_read_b32 v68, v25 offset:25856\n"
      "s_mov_b64 exec, s[30:31]\n"
      "ds_read_b32 v69, v27 offset:25856\n"
      "s_mov_b64 exec, s[34:35]\n"
      "ds_read_b32 v70, v29 offset:25856\n"
      "s_mov_b64 exec, s[36:37]\n"
      "ds_read_b32 v71, v31 offset:25856\n"
      "s_mov_b64 exec, s[38:39]\n"
      "ds_read_b32 v72, v33 offset:25856\n"
      "s_mov_b64 exec, s[40:41]\n"
      "ds_read_b32 v73, v35 offset:25856\n"
      "s_mov_b64 exec, s[42:43]\n"
      "ds_read_b32 v74, v37 offset:25856\n"
      "s_mov_b64 exec, s[44:45]\n"
      "ds_read_b32 v75, v39 offset:25856\n"
      "s_mov_b64 exec, s[46:47]\n"
      "ds_read_b32 v76, v41 offset:25856\n"
      "s_mov_b64 exec, s[48:49]\n"
      "ds_read_b32 v77, v43 offset:25856\n"
      "s_mov_b64 exec, s[50:51]\n"
      "ds_read_b32 v78, v45 offset:25856\n"
      "s_mov_b64 exec, s[92:93]\n"
      "ds_read_b32 v79, v47 offset:25856\n"
      "s_bfm_b64 exec, 49, 0\n"
      "s_mul_i32 s83, s60, %[w]\n"
      "s_add_u32 s84, s83, s60\n"
      "s_lshr_b32 s83, s83, 3\n"
      "s_lshr_b32 s84, s84, 3\n"
      "s_mov_b32 s63, 0\n"
      "s_mov_b32 s89, 0\n"
      "v_cmp_gt_u32 vcc, s83, v180\n"
      "v_cmp_gt_u32_e64 s[90:91], s84, v180\n"
      "s_bcnt1_i32_b64 s95, vcc\n"
      "s_add_u32 s63, s63, s95\n"
      "s_bcnt1_i32_b64 s95, s[90:91]\n"
      "s_add_u32 s89, s89, s95\n"
      "v_cmp_gt_u32 vcc, s83, v181\n"
      "v_cmp_gt_u32_e64 s[90:91], s84, v181\n"
      "s_bcnt1_i32_b64 s95, vcc\n"
      "s_add_u32 s63, s63, s95\n"
      "s_bcnt1_i32_b64 s95, s[90:91]\n"
      "s_add_u32 s89, s89, s95\n"
      "v_cmp_gt_u32 vcc, s83, v182\n"
      "v_cmp_gt_u32_e64 s[90:91], s84, v182\n"
      "s_bcnt1_i32_b64 s95, vcc\n"
      "s_add_u32 s63, s63, s95\n"
      "s_bcnt1_i32_b64 s95, s[90:91]\n"
      "s_add_u32 s89, s89, s95\n"
      "v_cmp_gt_u32 vcc, s83, v183\n"
      "v_cmp_gt_u32_e64 s[90:91], s84, v183\n"
      "s_bcnt1_i32_b64 s95, vcc\n"
      "s_add_u32 s63, s63, s95\n"
      "s_bcnt1_i32_b64 s95, s[90:91]\n"
      "s_add_u32 s89, s89, s95\n"
      "s_mov_b64 exec, -1\n"
      "s_cmp_eq_u32 %[w], 7\n"
      "s_cselect_b32 s89, %[rb], s89\n"
      "s_lshl_b32 s83, s63, 2\n"
      "s_lshl_b32 s84, s89, 2\n"
      "v_mov_b32 v80, s83\n"
      "v_mov_b32 v81, s84\n"
      "ds_read_b32 v80, v80 offset:25856\n"
      "ds_read_b32 v81, v81 offset:25856\n"
      "v_lshlrev_b32 v82, 3, v208\n"
      "s_add_u32 s83, s63, 8\n"
      "v_add_u32 v82, s83, v82\n"
      "v_min_u32 v82, s89, v82\n"
      "v_lshlrev_b32 v82, 2, v82\n"
      "ds_read_b32 v217, v82 offset:25856\n"
      "s_waitcnt lgkmcnt(15)\n"
      "v_add_u32 v64, v64, v48\n"
      "v_lshrrev_b32 v17, 2, v17\n"
      "v_lshlrev_b32 v48, 2, v64\n"
      "v_add_u32 v65, v65, v49\n"
      "v_lshrrev_b32 v19, 2, v19\n"
      "v_lshlrev_b32 v49, 2, v65\n"
      "v_add_u32 v66, v66, v50\n"
      "v_lshrrev_b32 v21, 2, v21\n"
      "v_lshlrev_b32 v50, 2, v66\n"
      "v_add_u32 v67, v67, v51\n"
      "v_lshrrev_b32 v23, 2, v23\n"
      "v_lshlrev_b32 v51, 2, v67\n"
      "s_waitcnt lgkmcnt(14)\n"
      "v_add_u32 v68, v68, v52\n"
      "v_lshrrev_b32 v25, 2, v25\n"
      "v_lshlrev_b32 v52, 2, v68\n"
      "s_waitcnt lgkmcnt(13)\n"
      "v_add_u32 v69, v69, v53\n"
      "v_lshrrev_b32 v27, 2, v27\n"
      "v_lshlrev_b32 v53, 2, v69\n"
      "s_waitcnt lgkmcnt(12)\n"
      "v_add_u32 v70, v70, v54\n"
      "v_lshrrev_b32 v29, 2, v29\n"
      "v_lshlrev_b32 v54, 2, v70\n"
      "s_waitcnt lgkmcnt(11)\n"
      "v_add_u32 v71, v71, v55\n"
      "v_lshrrev_b32 v31, 2, v31\n"
      "v_lshlrev_b32 v55, 2, v71\n"
      "s_waitcnt lgkmcnt(10)\n"
      "v_add_u32 v72, v72, v56\n"
      "v_lshrrev_b32 v33, 2, v33\n"
      "v_lshlrev_b32 v56, 2, v72\n"
      "s_waitcnt lgkmcnt(9)\n"
      "v_add_u32 v73, v73, v57\n"
      "v_lshrrev_b32 v35, 2, v35\n"
      "v_lshlrev_b32 v57, 2, v73\n"
      "s_waitcnt lgkmcnt(8)\n"
      "v_add_u32 v74, v74, v58\n"
      "v_lshrrev_b32 v37, 2, v37\n"
      "v_lshlrev_b32 v58, 2, v74\n"
      "s_waitcnt lgkmcnt(7)\n"
      "v_add_u32 v75, v75, v59\n"
      "v_lshrrev_b32 v39, 2, v39\n"
      "v_lshlrev_b32 v59, 2, v75\n"
      "s_waitcnt lgkmcnt(6)\n"
      "v_add_u32 v76, v76, v60\n"
      "v_lshrrev_b32 v41, 2, v41\n"
      "v_lshlrev_b32 v60, 2, v76\n"
      "s_waitcnt lgkmcnt(5)\n"
      "v_add_u32 v77, v77, v61\n"
      "v_lshrrev_b32 v43, 2, v43\n"
      "v_lshlrev_b32 v61, 2, v77\n"
      "s_waitcnt lgkmcnt(4)\n"
      "v_add_u32 v78, v78, v62\n"
      "v_lshrrev_b32 v45, 2, v45\n"
      "v_lshlrev_b32 v62, 2, v78\n"
      "s_waitcnt lgkmcnt(3)\n"
      "v_add_u32 v79, v79, v63\n"
      "v_lshrrev_b32 v47, 2, v47\n"
      "v_lshlrev_b32 v63, 2, v79\n"
      "s_mov_b64 exec, s[20:21]\n"
      "ds_write_b32 v48, v16 offset:0\n"
      "ds_write_b8 v64, v17 offset:17408\n"
      "s_mov_b64 exec, s[22:23]\n"
      "ds_write_b32 v49, v18 offset:0\n"
      "ds_write_b8 v65, v19 offset:17408\n"
      "s_mov_b64 exec, s[24:25]\n"
      "ds_write_b32 v50, v20 offset:0\n"
      "ds_write_b8 v66, v21 offset:17408\n"
      "s_mov_b64 exec, s[26:27]\n"
      "ds_write_b32 v51, v22 offset:0\n"
      "ds_write_b8 v67, v23 offset:17408\n"
      "s_mov_b64 exec, s[28:29]\n"
      "ds_write_b32 v52, v24 offset:0\n"
      "ds_write_b8 v68, v25 offset:17408\n"
      "s_mov_b64 exec, s[30:31]\n"
      "ds_write_b32 v53, v26 offset:0\n"
      "ds_write_b8 v69, v27 offset:17408\n"
      "s_mov_b64 exec, s[34:35]\n"
      "ds_write_b32 v54, v28 offset:0\n"
      "ds_write_b8 v70, v29 offset:17408\n"
      "s_mov_b64 exec, s[36:37]\n"
      "ds_write_b32 v55, v30 offset:0\n"
      "ds_write_b8 v71, v31 offset:17408\n"
      "s_mov_b64 exec, s[38:39]\n"
      "ds_write_b32 v56, v32 offset:0\n"
      "ds_write_b8 v72, v33 offset:17408\n"
      "s_mov_b64 exec, s[40:41]\n"
      "ds_write_b32 v57, v34 offset:0\n"
      "ds_write_b8 v73, v35 offset:17408\n"
      "s_mov_b64 exec, s[42:43]\n"
      "ds_write_b32 v58, v36 offset:0\n"
      "ds_write_b8 v74, v37 offset:17408\n"
      "s_mov_b64 exec, s[44:45]\n"
      "ds_write_b32 v59, v38 offset:0\n"
      "ds_write_b8 v75, v39 offset:17408\n"
      "s_mov_b64 exec, s[46:47]\n"
      "ds_write_b32 v60, v40 offset:0\n"
      "ds_write_b8 v76, v41 offset:17408\n"
      "s_mov_b64 exec, s[48:49]\n"
      "ds_write_b32 v61, v42 offset:0\n"
      "ds_write_b8 v77, v43 offset:17408\n"
      "s_mov_b64 exec, s[50:51]\n"
      "ds_write_b32 v62, v44 offset:0\n"
      "ds_write_b8 v78, v45 offset:17408\n"
      "s_mov_b64 exec, s[92:93]\n"
      "ds_write_b32 v63, v46 offset:0\n"
      "ds_write_b8 v79, v47 offset:17408\n"
      "s_mov_b64 exec, -1\n"
      "s_mul_i32 s83, %[w], 24\n"
      "s_add_u32 s83, s83, s60\n"
      "v_add_u32 v82, s83, v208\n"
      "v_lshlrev_b32 v83, 2, v82\n"
      "v_mov_b32 v84, %[rb]\n"
      "s_mov_b64 exec, 0xffffff\n"
      "ds_write_b32 v83, v220 offset:0\n"
      "ds_write_b8 v82, v84 offset:17408\n"
      "s_mov_b64 exec, -1\n"
      "s_waitcnt lgkmcnt(15)\n"
      "v_readfirstlane_b32 s61, v80\n"
      "v_readfirstlane_b32 s62, v81\n"
      "s_waitcnt vmcnt(4)\n"
      "s_nop 0\n"
      "s_waitcnt lgkmcnt(0)\n"
      "s_barrier\n"
      "s_sub_u32 s72, s62, s61\n"
      "s_add_u32 s72, s72, 31\n"
      "s_lshr_b32 s72, s72, 5\n"
      "s_mov_b32 s73, s61\n"
      "s_mov_b32 s75, s63\n"
      "v_readlane_b32 s74, v217, 0\n"
      "s_lshl_b32 s83, s61, 2\n"
      "s_add_u32 s83, s83, 0\n"
      "v_lshl_add_u32 v229, v208, 2, s83\n"
      "s_add_u32 s84, s61, 17408\n"
      "v_add_u32 v230, s84, v208\n"
      "v_lshrrev_b32 v95, 3, v208\n"
      "v_lshl_add_u32 v228, v95, 2, s83\n"
      "ds_read2_b32 v[210:211], v228 offset0:0 offset1:8\n"
      "ds_read2_b32 v[212:213], v228 offset0:16 offset1:24\n"
      "ds_read2st64_b32 v[218:219], v229 offset0:0 offset1:1\n"
      "ds_read_u8 v209, v230\n"
      "s_add_u32 m0, s79, 0\n"
      "s_waitcnt lgkmcnt(3)\n"
      "v_mad_u32_u16 v210, v210, s80, v226\n"
      "buffer_load_dwordx4 v210, s[64:67], 0 offen lds\n"
      "s_add_u32 m0, s79, 1024\n"
      "v_mad_u32_u16 v211, v211, s80, v227\n"
      "buffer_load_dwordx4 v211, s[64:67], 0 offen lds\n"
      "s_add_u32 m0, s79, 2048\n"
      "s_waitcnt lgkmcnt(2)\n"
      "v_mad_u32_u16 v212, v212, s80, v226\n"
      "buffer_load_dwordx4 v212, s[64:67], 0 offen lds\n"
      "s_add_u32 m0, s79, 3072\n"
      "v_mad_u32_u16 v213, v213, s80, v227\n"
      "buffer_load_dwordx4 v213, s[64:67], 0 offen lds\n"
      "s_waitcnt lgkmcnt(1)\n"
      "v_mad_u32_u16 v95, v218, 16, 0\n"
      "buffer_load_dwordx3 v[214:216], v95, s[68:71], 0 offen\n"
      "ds_read2_b32 v[210:211], v228 offset0:32 offset1:40\n"
      "ds_read2_b32 v[212:213], v228 offset0:48 offset1:56\n"
      "ds_read_b128 v[16:19], v241 offset:33024\n"
      "ds_read_b128 v[20:23], v241 offset:34048\n"
      "ds_read_b128 v[24:27], v241 offset:35072\n"
      "ds_read_b128 v[28:31], v241 offset:36096\n"
      "ds_read_b128 v[32:35], v241 offset:37120\n"
      "ds_read_b128 v[36:39], v241 offset:38144\n"
      "ds_read_b128 v[40:43], v241 offset:39168\n"
      "ds_read_b128 v[44:47], v241 offset:40192\n"
      "v_add_u32 v241, 152448, v241\n"
      "ds_read_b128 v[144:147], v241 offset:0\n"
      "ds_read_b128 v[148:151], v241 offset:1024\n"
      "ds_read_b128 v[152:155], v241 offset:2048\n"
      "ds_read_b128 v[156:159], v241 offset:3072\n"
      "ds_read_b128 v[160:163], v241 offset:4096\n"
      "ds_read_b128 v[164:167], v241 offset:5120\n"
      "ds_read_b128 v[168:171], v241 offset:6144\n"
      "ds_read_b128 v[172:175], v241 offset:7168\n"
      "s_waitcnt vmcnt(8) lgkmcnt(15)\n"
      "v_mfma_f32_32x32x16_bf16 v[48:63], v[16:19], v[0:3], 0\n"
      "s_waitcnt lgkmcnt(11)\n"
      "v_mfma_f32_32x32x16_bf16 v[64:79], v[32:35], v[0:3], 0\n"
      "s_waitcnt vmcnt(7)\n"
      "v_mfma_f32_32x32x16_bf16 v[48:63], v[20:23], v[4:7], v[48:63]\n"
      "s_waitcnt lgkmcnt(10)\n"
      "v_mfma_f32_32x32x16_bf16 v[64:79], v[36:39], v[4:7], v[64:79]\n"
      "s_waitcnt vmcnt(6)\n"
      "v_mfma_f32_32x32x16_bf16 v[48:63], v[24:27], v[8:11], v[48:63]\n"
      "s_waitcnt lgkmcnt(9)\n"
      "v_mfma_f32_32x32x16_bf16 v[64:79], v[40:43], v[8:11], v[64:79]\n"
      "s_waitcnt vmcnt(5)\n"
      "v_mfma_f32_32x32x16_bf16 v[48:63], v[28:31], v[12:15], v[48:63]\n"
      "s_waitcnt lgkmcnt(8)\n"
      "v_mfma_f32_32x32x16_bf16 v[64:79], v[44:47], v[12:15], v[64:79]\n"
      "s_nop 9\n"
      "v_mul_f32 v48, 0xbfb8aa3b, v48\n"
      "v_mul_f32 v49, 0xbfb8aa3b, v49\n"
      "v_mul_f32 v50, 0xbfb8aa3b, v50\n"
      "v_mul_f32 v51, 0xbfb8aa3b, v51\n"
      "v_mul_f32 v52, 0xbfb8aa3b, v52\n"
      "v_mul_f32 v53, 0xbfb8aa3b, v53\n"
      "v_mul_f32 v54, 0xbfb8aa3b, v54\n"
      "v_mul_f32 v55, 0xbfb8aa3b, v55\n"
      "v_mul_f32 v56, 0xbfb8aa3b, v56\n"
      "v_mul_f32 v57, 0xbfb8aa3b, v57\n"
      "v_mul_f32 v58, 0xbfb8aa3b, v58\n"
      "v_mul_f32 v59, 0xbfb8aa3b, v59\n"
      "v_mul_f32 v60, 0xbfb8aa3b, v60\n"
      "v_mul_f32 v61, 0xbfb8aa3b, v61\n"
      "v_mul_f32 v62, 0xbfb8aa3b, v62\n"
      "v_mul_f32 v63, 0xbfb8aa3b, v63\n"
      "v_mul_f32 v64, 0xbfb8aa3b, v64\n"
      "v_mul_f32 v65, 0xbfb8aa3b, v65\n"
      "v_mul_f32 v66, 0xbfb8aa3b, v66\n"
      "v_mul_f32 v67, 0xbfb8aa3b, v67\n"
      "v_mul_f32 v68, 0xbfb8aa3b, v68\n"
      "v_mul_f32 v69, 0xbfb8aa3b, v69\n"
      "v_mul_f32 v70, 0xbfb8aa3b, v70\n"
      "v_mul_f32 v71, 0xbfb8aa3b, v71\n"
      "v_mul_f32 v72, 0xbfb8aa3b, v72\n"
      "v_mul_f32 v73, 0xbfb8aa3b, v73\n"
      "v_mul_f32 v74, 0xbfb8aa3b, v74\n"
      "v_mul_f32 v75, 0xbfb8aa3b, v75\n"
      "v_mul_f32 v76, 0xbfb8aa3b, v76\n"
      "v_mul_f32 v77, 0xbfb8aa3b, v77\n"
      "v_mul_f32 v78, 0xbfb8aa3b, v78\n"
      "v_mul_f32 v79, 0xbfb8aa3b, v79\n"
      "v_cvt_pk_bf16_f32 v80, v48, v49\n"
      "v_cvt_pk_bf16_f32 v81, v50, v51\n"
      "v_cvt_pk_bf16_f32 v82, v52, v53\n"
      "v_cvt_pk_bf16_f32 v83, v54, v55\n"
      "v_cvt_pk_bf16_f32 v84, v56, v57\n"
      "v_cvt_pk_bf16_f32 v85, v58, v59\n"
      "v_cvt_pk_bf16_f32 v86, v60, v61\n"
      "v_cvt_pk_bf16_f32 v87, v62, v63\n"
      "v_cvt_pk_bf16_f32 v88, v64, v65\n"
      "v_cvt_pk_bf16_f32 v89, v66, v67\n"
      "v_cvt_pk_bf16_f32 v90, v68, v69\n"
      "v_cvt_pk_bf16_f32 v91, v70, v71\n"
      "v_cvt_pk_bf16_f32 v92, v72, v73\n"
      "v_cvt_pk_bf16_f32 v93, v74, v75\n"
      "v_cvt_pk_bf16_f32 v94, v76, v77\n"
      "v_cvt_pk_bf16_f32 v95, v78, v79\n"
      "s_mov_b64 exec, s[56:57]\n"
      "ds_write_b128 v242, v[80:83]\n"
      "ds_write_b128 v242, v[84:87] offset:16\n"
      "ds_write_b128 v242, v[88:91] offset:64\n"
      "ds_write_b128 v242, v[92:95] offset:80\n"
      "s_mov_b64 exec, -1\n"
      "s_add_u32 m0, s79, 4096\n"
      "v_mad_u32_u16 v210, v210, s80, v226\n"
      "buffer_load_dwordx4 v210, s[64:67], 0 offen lds\n"
      "s_add_u32 m0, s79, 5120\n"
      "v_mad_u32_u16 v211, v211, s80, v227\n"
      "buffer_load_dwordx4 v211, s[64:67], 0 offen lds\n"
      "s_add_u32 m0, s79, 6144\n"
      "v_mad_u32_u16 v212, v212, s80, v226\n"
      "buffer_load_dwordx4 v212, s[64:67], 0 offen lds\n"
      "s_add_u32 m0, s79, 7168\n"
      "v_mad_u32_u16 v213, v213, s80, v227\n"
      "buffer_load_dwordx4 v213, s[64:67], 0 offen lds\n"
      "s_waitcnt lgkmcnt(0)\n"
      "s_barrier\n"
      "s_cmp_eq_u32 s72, 0\n"
      "s_cbranch_scc1 LN0_%=\n"
      "v_min_u32 v182, s89, v209\n"
      "v_lshl_add_u32 v184, v182, 4, s85\n"
      "ds_read_b128 v[176:179], v184\n"
      "s_waitcnt vmcnt(4) lgkmcnt(0)\n"
      "v_pk_add_f32 v[176:177], v[176:177], v[214:215] neg_lo:[0,1] neg_hi:[0,1]\n"
      "v_sub_f32 v178, v178, v216\n"
      "v_cvt_pk_bf16_f32 v180, 1.0, v176\n"
      "v_cvt_pk_bf16_f32 v181, v177, v178\n"
      "v_mad_u32_u16 v184, v219, 16, 0\n"
      "v_and_b32 v176, 0xffff0000, v180\n"
      "v_lshlrev_b32 v177, 16, v181\n"
      "v_and_b32 v178, 0xffff0000, v181\n"
      "buffer_load_dwordx3 v[214:216], v184, s[68:71], 0 offen\n"
      "v_mul_f32 v176, v176, v176\n"
      "v_fmac_f32 v176, v177, v177\n"
      "v_fmac_f32 v176, v178, v178\n"
      "v_add_u32 v229, 0x100, v229\n"
      "v_cvt_pk_bf16_f32 v176, v176, v176\n"
      "v_add_u32 v230, 64, v230\n"
      "v_bfi_b32 v183, s86, v176, v218\n"
      "ds_write_b128 v238, v[180:183]\n"
      "ds_read2_b32 v[210:211], v228 offset0:64 offset1:72\n"
      "ds_read2_b32 v[212:213], v228 offset0:80 offset1:88\n"
      "ds_read_b128 v[176:179], v222 offset:0\n"
      "ds_read_b128 v[180:183], v223 offset:0\n"
      "ds_read_b128 v[184:187], v224 offset:0\n"
      "ds_read_b128 v[188:191], v225 offset:0\n"
      "ds_read_b32 v208, v231 offset:8\n"
      "s_waitcnt lgkmcnt(0)\n"
      "v_readfirstlane_b32 s77, v208\n"
      "s_lshl_b32 s78, s77, 3\n"
      "s_nop 0\n"
      "v_subrev_u32 v200, s77, v208\n"
      "v_cmp_le_u32 vcc, 8, v200\n"
      "s_cbranch_vccnz LR1P_%=\n"
      "LB1P_%=:\n"
      "v_mad_u32_u24 v200, v200, v234, v232\n"
      "v_mad_u32_u24 v198, s78, v234, v233\n"
      "ds_read_b128 v[200:203], v200 offset:0\n"
      "ds_read_b64_tr_b16 v[192:193], v198 offset:0\n"
      "ds_read_b64_tr_b16 v[194:195], v198 offset:512\n"
      "ds_read_b64_tr_b16 v[196:197], v198 offset:64\n"
      "ds_read_b64_tr_b16 v[198:199], v198 offset:576\n"
      "v_mfma_f32_32x32x16_bf16 v[0:15], v[176:179], v[144:147], 0\n"
      "v_mfma_f32_32x32x16_bf16 v[16:31], v[176:179], v[160:163], 0\n"
      "v_mfma_f32_32x32x16_bf16 v[0:15], v[180:183], v[148:151], v[0:15]\n"
      "v_mfma_f32_32x32x16_bf16 v[16:31], v[180:183], v[164:167], v[16:31]\n"
      "v_mfma_f32_32x32x16_bf16 v[0:15], v[184:187], v[152:155], v[0:15]\n"
      "v_mfma_f32_32x32x16_bf16 v[16:31], v[184:187], v[168:171], v[16:31]\n"
      "v_mfma_f32_32x32x16_bf16 v[0:15], v[188:191], v[156:159], v[0:15]\n"
      "v_mfma_f32_32x32x16_bf16 v[16:31], v[188:191], v[172:175], v[16:31]\n"
      "s_add_u32 m0, s79, 0\n"
      "v_mad_u32_u16 v210, v210, s80, v226\n"
      "buffer_load_dwordx4 v210, s[64:67], 0 offen lds\n"
      "s_add_u32 m0, s79, 1024\n"
      "v_mad_u32_u16 v211, v211, s80, v227\n"
      "buffer_load_dwordx4 v211, s[64:67], 0 offen lds\n"
      "s_add_u32 m0, s79, 2048\n"
      "v_mad_u32_u16 v212, v212, s80, v226\n"
      "buffer_load_dwordx4 v212, s[64:67], 0 offen lds\n"
      "s_add_u32 m0, s79, 3072\n"
      "v_mad_u32_u16 v213, v213, s80, v227\n"
      "buffer_load_dwordx4 v213, s[64:67], 0 offen lds\n"
      "s_waitcnt lgkmcnt(2)\n"
      "v_mfma_f32_32x32x16_bf16 v[0:15], v[200:203], v[192:195], v[0:15]\n"
      "s_waitcnt lgkmcnt(0)\n"
      "v_mfma_f32_32x32x16_bf16 v[16:31], v[200:203], v[196:199], v[16:31]\n"
      "s_cmp_lg_u32 s81, 0\n"
      "s_cbranch_scc1 LR2P_%=\n"
      "LB2P_%=:\n"
      "v_add_u32 v228, 0x180, v228\n"
      "v_mov_b32 v192, 0\n"
      "v_mov_b32 v193, 0\n"
      "v_mov_b32 v194, 0\n"
      "v_mov_b32 v195, 0\n"
      "v_mov_b32 v196, 0\n"
      "v_mov_b32 v197, 0\n"
      "v_mov_b32 v198, 0\n"
      "v_mov_b32 v199, 0\n"
      "v_mov_b32 v204, 0\n"
      "v_mov_b32 v205, 0\n"
      "v_mov_b32 v206, 0xffffff\n"
      "s_mul_i32 s58, s72, 8\n"
      "s_lshr_b32 s58, s58, 4\n"
      "s_setprio 1\n"
      "s_cmp_lt_u32 %[w], 4\n"
      "s_cselect_b32 s58, s58, 0\n"
      "s_cbranch_scc0 LSQ_%=\n"
      "s_setprio 2\n"
      "LSQ_%=:\n"
      "LIE_%=:\n"
      "s_waitcnt vmcnt(5)\n"
      "ds_read_b128 v[176:179], v222 offset:4096\n"
      "ds_read_b128 v[180:183], v223 offset:4096\n"
      "ds_read_b128 v[184:187], v224 offset:4096\n"
      "ds_read_b128 v[188:191], v225 offset:4096\n"
      "ds_read_b32 v208, v231 offset:520\n"
      "ds_read2_b32 v[210:211], v228 offset0:0 offset1:8\n"
      "ds_read2_b32 v[212:213], v228 offset0:16 offset1:24\n"
      "ds_read2st64_b32 v[218:219], v229 offset0:0 offset1:1\n"
      "ds_read_u8 v209, v230\n"
      "s_waitcnt lgkmcnt(12)\n"
      "v_mfma_f32_32x32x16_bf16 v[96:111], v[128:131], v[192:195], v[96:111]\n"
      "v_exp_f32 v64, v0\n"
      "v_exp_f32 v65, v1\n"
      "v_exp_f32 v66, v2\n"
      "v_mfma_f32_32x32x16_bf16 v[112:127], v[136:139], v[192:195], v[112:127]\n"
      "v_exp_f32 v67, v3\n"
      "v_exp_f32 v68, v4\n"
      "v_exp_f32 v69, v5\n"
      "s_waitcnt lgkmcnt(10)\n"
      "v_mfma_f32_32x32x16_bf16 v[96:111], v[132:135], v[196:199], v[96:111]\n"
      "v_exp_f32 v70, v6\n"
      "v_exp_f32 v71, v7\n"
      "v_exp_f32 v72, v8\n"
      "v_mfma_f32_32x32x16_bf16 v[112:127], v[140:143], v[196:199], v[112:127]\n"
      "v_exp_f32 v73, v9\n"
      "v_exp_f32 v74, v10\n"
      "v_exp_f32 v75, v11\n"
      "s_cmp_le_i32 s74, s73\n"
      "s_cbranch_scc1 LFE_%=\n"
      "LBFE_%=:\n"
      "s_waitcnt lgkmcnt(4)\n"
      "v_readfirstlane_b32 s77, v208\n"
      "s_lshl_b32 s78, s77, 3\n"
      "s_add_u32 s73, s73, 32\n"
      "v_subrev_u32 v204, s77, v208\n"
      "v_cmp_le_u32 vcc, 8, v204\n"
      "s_cbranch_vccnz LR1E_%=\n"
      "LB1E_%=:\n"
      "v_mad_u32_u24 v204, v204, v234, v232\n"
      "v_mad_u32_u24 v198, s78, v234, v233\n"
      "ds_read_b128 v[204:207], v204 offset:512\n"
      "ds_read_b64_tr_b16 v[192:193], v198 offset:0\n"
      "ds_read_b64_tr_b16 v[194:195], v198 offset:512\n"
      "ds_read_b64_tr_b16 v[196:197], v198 offset:64\n"
      "ds_read_b64_tr_b16 v[198:199], v198 offset:576\n"
      "v_mfma_f32_32x32x16_bf16 v[32:47], v[176:179], v[144:147], 0\n"
      "v_exp_f32 v76, v12\n"
      "v_exp_f32 v77, v13\n"
      "v_exp_f32 v78, v14\n"
      "v_mfma_f32_32x32x16_bf16 v[48:63], v[176:179], v[160:163], 0\n"
      "v_exp_f32 v79, v15\n"
      "v_exp_f32 v80, v16\n"
      "v_exp_f32 v81, v17\n"
      "v_mfma_f32_32x32x16_bf16 v[32:47], v[180:183], v[148:151], v[32:47]\n"
      "v_exp_f32 v82, v18\n"
      "v_exp_f32 v83, v19\n"
      "v_exp_f32 v84, v20\n"
      "v_mfma_f32_32x32x16_bf16 v[48:63], v[180:183], v[164:167], v[48:63]\n"
      "v_exp_f32 v85, v21\n"
      "v_exp_f32 v86, v22\n"
      "v_exp_f32 v87, v23\n"
      "v_exp_f32 v88, v24\n"
      "v_exp_f32 v89, v25\n"
      "v_exp_f32 v90, v26\n"
      "v_exp_f32 v91, v27\n"
      "v_exp_f32 v92, v28\n"
      "v_exp_f32 v93, v29\n"
      "v_exp_f32 v94, v30\n"
      "v_exp_f32 v95, v31\n"
      "s_add_u32 m0, s79, 4096\n"
      "s_waitcnt lgkmcnt(8)\n"
      "v_mad_u32_u16 v210, v210, s80, v226\n"
      "buffer_load_dwordx4 v210, s[64:67], 0 offen lds\n"
      "s_add_u32 m0, s79, 5120\n"
      "v_mad_u32_u16 v211, v211, s80, v227\n"
      "buffer_load_dwordx4 v211, s[64:67], 0 offen lds\n"
      "s_add_u32 m0, s79, 6144\n"
      "s_waitcnt lgkmcnt(7)\n"
      "v_mad_u32_u16 v212, v212, s80, v226\n"
      "buffer_load_dwordx4 v212, s[64:67], 0 offen lds\n"
      "s_add_u32 m0, s79, 7168\n"
      "v_mad_u32_u16 v213, v213, s80, v227\n"
      "buffer_load_dwordx4 v213, s[64:67], 0 offen lds\n"
      "v_pk_add_f32 v[64:65], v[64:65], 1.0 op_sel_hi:[1,0]\n"
      "v_pk_add_f32 v[66:67], v[66:67], 1.0 op_sel_hi:[1,0]\n"
      "v_pk_add_f32 v[68:69], v[68:69], 1.0 op_sel_hi:[1,0]\n"
      "v_pk_add_f32 v[70:71], v[70:71], 1.0 op_sel_hi:[1,0]\n"
      "v_pk_add_f32 v[72:73], v[72:73], 1.0 op_sel_hi:[1,0]\n"
      "v_pk_add_f32 v[74:75], v[74:75], 1.0 op_sel_hi:[1,0]\n"
      "v_pk_add_f32 v[76:77], v[76:77], 1.0 op_sel_hi:[1,0]\n"
      "v_pk_add_f32 v[78:79], v[78:79], 1.0 op_sel_hi:[1,0]\n"
      "v_pk_add_f32 v[80:81], v[80:81], 1.0 op_sel_hi:[1,0]\n"
      "v_pk_add_f32 v[82:83], v[82:83], 1.0 op_sel_hi:[1,0]\n"
      "v_pk_add_f32 v[84:85], v[84:85], 1.0 op_sel_hi:[1,0]\n"
      "v_pk_add_f32 v[86:87], v[86:87], 1.0 op_sel_hi:[1,0]\n"
      "v_pk_add_f32 v[88:89], v[88:89], 1.0 op_sel_hi:[1,0]\n"
      "v_pk_add_f32 v[90:91], v[90:91], 1.0 op_sel_hi:[1,0]\n"
      "v_pk_add_f32 v[92:93], v[92:93], 1.0 op_sel_hi:[1,0]\n"
      "v_pk_add_f32 v[94:95], v[94:95], 1.0 op_sel_hi:[1,0]\n"
      "v_mfma_f32_32x32x16_bf16 v[32:47], v[184:187], v[152:155], v[32:47]\n"
      "v_rcp_f32 v64, v64\n"
      "v_rcp_f32 v65, v65\n"
      "v_rcp_f32 v66, v66\n"
      "v_mfma_f32_32x32x16_bf16 v[48:63], v[184:187], v[168:171], v[48:63]\n"
      "v_rcp_f32 v67, v67\n"
      "v_rcp_f32 v68, v68\n"
      "v_rcp_f32 v69, v69\n"
      "v_mfma_f32_32x32x16_bf16 v[32:47], v[188:191], v[156:159], v[32:47]\n"
      "v_rcp_f32 v70, v70\n"
      "v_rcp_f32 v71, v71\n"
      "v_rcp_f32 v72, v72\n"
      "v_mfma_f32_32x32x16_bf16 v[48:63], v[188:191], v[172:175], v[48:63]\n"
      "v_rcp_f32 v73, v73\n"
      "v_rcp_f32 v74, v74\n"
      "v_rcp_f32 v75, v75\n"
      "s_waitcnt lgkmcnt(2)\n"
      "v_mfma_f32_32x32x16_bf16 v[32:47], v[204:207], v[192:195], v[32:47]\n"
      "v_rcp_f32 v76, v76\n"
      "v_rcp_f32 v77, v77\n"
      "v_rcp_f32 v78, v78\n"
      "s_waitcnt lgkmcnt(0)\n"
      "v_mfma_f32_32x32x16_bf16 v[48:63], v[204:207], v[196:199], v[48:63]\n"
      "v_rcp_f32 v79, v79\n"
      "v_rcp_f32 v80, v80\n"
      "v_rcp_f32 v81, v81\n"
      "s_cmp_lg_u32 s81, 0\n"
      "s_cbranch_scc1 LR2E_%=\n"
      "LB2E_%=:\n"
      "v_subrev_u32 v208, s75, v202\n"
      "v_min_u32 v208, 8, v208\n"
      "v_mad_u32_u24 v208, v208, v235, v236\n"
      "ds_write_b64 v208, v[200:201]\n"
      "ds_read_b64_tr_b16 v[192:193], v237 offset:0\n"
      "ds_read_b64_tr_b16 v[194:195], v237 offset:576\n"
      "ds_read_b64_tr_b16 v[196:197], v237 offset:1152\n"
      "ds_read_b64_tr_b16 v[198:199], v237 offset:1728\n"
      "ds_write_b64 v208, v[220:221]\n"
      "v_min_u32 v182, s89, v209\n"
      "v_lshl_add_u32 v184, v182, 4, s85\n"
      "ds_read_b128 v[176:179], v184\n"
      "v_rcp_f32 v82, v82\n"
      "v_rcp_f32 v83, v83\n"
      "v_rcp_f32 v84, v84\n"
      "v_rcp_f32 v85, v85\n"
      "v_rcp_f32 v86, v86\n"
      "v_rcp_f32 v87, v87\n"
      "v_rcp_f32 v88, v88\n"
      "v_rcp_f32 v89, v89\n"
      "v_rcp_f32 v90, v90\n"
      "v_rcp_f32 v91, v91\n"
      "v_rcp_f32 v92, v92\n"
      "v_rcp_f32 v93, v93\n"
      "v_rcp_f32 v94, v94\n"
      "v_rcp_f32 v95, v95\n"
      "v_pk_mul_f32 v[64:65], v[64:65], v[0:1]\n"
      "v_pk_mul_f32 v[66:67], v[66:67], v[2:3]\n"
      "v_pk_mul_f32 v[68:69], v[68:69], v[4:5]\n"
      "v_pk_mul_f32 v[70:71], v[70:71], v[6:7]\n"
      "v_pk_mul_f32 v[72:73], v[72:73], v[8:9]\n"
      "v_pk_mul_f32 v[74:75], v[74:75], v[10:11]\n"
      "v_pk_mul_f32 v[76:77], v[76:77], v[12:13]\n"
      "v_pk_mul_f32 v[78:79], v[78:79], v[14:15]\n"
      "v_pk_mul_f32 v[80:81], v[80:81], v[16:17]\n"
      "v_pk_mul_f32 v[82:83], v[82:83], v[18:19]\n"
      "v_pk_mul_f32 v[84:85], v[84:85], v[20:21]\n"
      "v_pk_mul_f32 v[86:87], v[86:87], v[22:23]\n"
      "v_pk_mul_f32 v[88:89], v[88:89], v[24:25]\n"
      "v_pk_mul_f32 v[90:91], v[90:91], v[26:27]\n"
      "v_pk_mul_f32 v[92:93], v[92:93], v[28:29]\n"
      "v_pk_mul_f32 v[94:95], v[94:95], v[30:31]\n"
      "s_waitcnt vmcnt(8) lgkmcnt(0)\n"
      "v_pk_add_f32 v[176:177], v[176:177], v[214:215] neg_lo:[0,1] neg_hi:[0,1]\n"
      "v_sub_f32 v178, v178, v216\n"
      "v_cvt_pk_bf16_f32 v180, 1.0, v176\n"
      "v_cvt_pk_bf16_f32 v181, v177, v178\n"
      "v_mad_u32_u16 v184, v219, 16, 0\n"
      "v_and_b32 v176, 0xffff0000, v180\n"
      "v_lshlrev_b32 v177, 16, v181\n"
      "v_and_b32 v178, 0xffff0000, v181\n"
      "buffer_load_dwordx3 v[214:216], v184, s[68:71], 0 offen\n"
      "v_mul_f32 v176, v176, v176\n"
      "v_fmac_f32 v176, v177, v177\n"
      "v_fmac_f32 v176, v178, v178\n"
      "v_add_u32 v229, 0x100, v229\n"
      "v_cvt_pk_bf16_f32 v176, v176, v176\n"
      "v_add_u32 v230, 64, v230\n"
      "v_bfi_b32 v183, s86, v176, v218\n"
      "ds_write_b128 v238, v[180:183]\n"
      "v_cvt_pk_bf16_f32 v128, v64, v65\n"
      "v_cvt_pk_bf16_f32 v129, v66, v67\n"
      "v_cvt_pk_bf16_f32 v130, v68, v69\n"
      "v_cvt_pk_bf16_f32 v131, v70, v71\n"
      "v_cvt_pk_bf16_f32 v132, v72, v73\n"
      "v_cvt_pk_bf16_f32 v133, v74, v75\n"
      "v_cvt_pk_bf16_f32 v134, v76, v77\n"
      "v_cvt_pk_bf16_f32 v135, v78, v79\n"
      "v_cvt_pk_bf16_f32 v136, v80, v81\n"
      "v_cvt_pk_bf16_f32 v137, v82, v83\n"
      "v_cvt_pk_bf16_f32 v138, v84, v85\n"
      "v_cvt_pk_bf16_f32 v139, v86, v87\n"
      "v_cvt_pk_bf16_f32 v140, v88, v89\n"
      "v_cvt_pk_bf16_f32 v141, v90, v91\n"
      "v_cvt_pk_bf16_f32 v142, v92, v93\n"
      "v_cvt_pk_bf16_f32 v143, v94, v95\n"
      "s_sub_u32 s72, s72, 1\n"
      "s_cmp_eq_u32 s72, 0\n"
      "s_cbranch_scc1 LTE_%=\n"
      "LIO_%=:\n"
      "s_waitcnt vmcnt(5)\n"
      "ds_read_b128 v[176:179], v222 offset:0\n"
      "ds_read_b128 v[180:183], v223 offset:0\n"
      "ds_read_b128 v[184:187], v224 offset:0\n"
      "ds_read_b128 v[188:191], v225 offset:0\n"
      "ds_read_b32 v208, v231 offset:8\n"
      "ds_read2_b32 v[210:211], v228 offset0:32 offset1:40\n"
      "ds_read2_b32 v[212:213], v228 offset0:48 offset1:56\n"
      "v_mfma_f32_32x32x16_bf16 v[96:111], v[128:131], v[192:195], v[96:111]\n"
      "v_exp_f32 v64, v32\n"
      "v_exp_f32 v65, v33\n"
      "v_exp_f32 v66, v34\n"
      "v_mfma_f32_32x32x16_bf16 v[112:127], v[136:139], v[192:195], v[112:127]\n"
      "v_exp_f32 v67, v35\n"
      "v_exp_f32 v68, v36\n"
      "v_exp_f32 v69, v37\n"
      "v_mfma_f32_32x32x16_bf16 v[96:111], v[132:135], v[196:199], v[96:111]\n"
      "v_exp_f32 v70, v38\n"
      "v_exp_f32 v71, v39\n"
      "v_exp_f32 v72, v40\n"
      "v_mfma_f32_32x32x16_bf16 v[112:127], v[140:143], v[196:199], v[112:127]\n"
      "v_exp_f32 v73, v41\n"
      "v_exp_f32 v74, v42\n"
      "v_exp_f32 v75, v43\n"
      "s_cmp_le_i32 s74, s73\n"
      "s_cbranch_scc1 LFO_%=\n"
      "LBFO_%=:\n"
      "s_waitcnt lgkmcnt(2)\n"
      "v_readfirstlane_b32 s77, v208\n"
      "s_lshl_b32 s78, s77, 3\n"
      "s_add_u32 s73, s73, 32\n"
      "v_subrev_u32 v200, s77, v208\n"
      "v_cmp_le_u32 vcc, 8, v200\n"
      "s_cbranch_vccnz LR1O_%=\n"
      "LB1O_%=:\n"
      "v_mad_u32_u24 v200, v200, v234, v232\n"
      "v_mad_u32_u24 v198, s78, v234, v233\n"
      "ds_read_b128 v[200:203], v200 offset:0\n"
      "ds_read_b64_tr_b16 v[192:193], v198 offset:0\n"
      "ds_read_b64_tr_b16 v[194:195], v198 offset:512\n"
      "ds_read_b64_tr_b16 v[196:197], v198 offset:64\n"
      "ds_read_b64_tr_b16 v[198:199], v198 offset:576\n"
      "v_mfma_f32_32x32x16_bf16 v[0:15], v[176:179], v[144:147], 0\n"
      "v_exp_f32 v76, v44\n"
      "v_exp_f32 v77, v45\n"
      "v_exp_f32 v78, v46\n"
      "v_mfma_f32_32x32x16_bf16 v[16:31], v[176:179], v[160:163], 0\n"
      "v_exp_f32 v79, v47\n"
      "v_exp_f32 v80, v48\n"
      "v_exp_f32 v81, v49\n"
      "v_mfma_f32_32x32x16_bf16 v[0:15], v[180:183], v[148:151], v[0:15]\n"
      "v_exp_f32 v82, v50\n"
      "v_exp_f32 v83, v51\n"
      "v_exp_f32 v84, v52\n"
      "v_mfma_f32_32x32x16_bf16 v[16:31], v[180:183], v[164:167], v[16:31]\n"
      "v_exp_f32 v85, v53\n"
      "v_exp_f32 v86, v54\n"
      "v_exp_f32 v87, v55\n"
      "v_exp_f32 v88, v56\n"
      "v_exp_f32 v89, v57\n"
      "v_exp_f32 v90, v58\n"
      "v_exp_f32 v91, v59\n"
      "v_exp_f32 v92, v60\n"
      "v_exp_f32 v93, v61\n"
      "v_exp_f32 v94, v62\n"
      "v_exp_f32 v95, v63\n"
      "s_add_u32 m0, s79, 0\n"
      "s_waitcnt lgkmcnt(6)\n"
      "v_mad_u32_u16 v210, v210, s80, v226\n"
      "buffer_load_dwordx4 v210, s[64:67], 0 offen lds\n"
      "s_add_u32 m0, s79, 1024\n"
      "v_mad_u32_u16 v211, v211, s80, v227\n"
      "buffer_load_dwordx4 v211, s[64:67], 0 offen lds\n"
      "s_add_u32 m0, s79, 2048\n"
      "s_waitcnt lgkmcnt(5)\n"
      "v_mad_u32_u16 v212, v212, s80, v226\n"
      "buffer_load_dwordx4 v212, s[64:67], 0 offen lds\n"
      "s_add_u32 m0, s79, 3072\n"
      "v_mad_u32_u16 v213, v213, s80, v227\n"
      "buffer_load_dwordx4 v213, s[64:67], 0 offen lds\n"
      "v_add_u32 v228, 0x100, v228\n"
      "v_pk_add_f32 v[64:65], v[64:65], 1.0 op_sel_hi:[1,0]\n"
      "v_pk_add_f32 v[66:67], v[66:67], 1.0 op_sel_hi:[1,0]\n"
      "v_pk_add_f32 v[68:69], v[68:69], 1.0 op_sel_hi:[1,0]\n"
      "v_pk_add_f32 v[70:71], v[70:71], 1.0 op_sel_hi:[1,0]\n"
      "v_pk_add_f32 v[72:73], v[72:73], 1.0 op_sel_hi:[1,0]\n"
      "v_pk_add_f32 v[74:75], v[74:75], 1.0 op_sel_hi:[1,0]\n"
      "v_pk_add_f32 v[76:77], v[76:77], 1.0 op_sel_hi:[1,0]\n"
      "v_pk_add_f32 v[78:79], v[78:79], 1.0 op_sel_hi:[1,0]\n"
      "v_pk_add_f32 v[80:81], v[80:81], 1.0 op_sel_hi:[1,0]\n"
      "v_pk_add_f32 v[82:83], v[82:83], 1.0 op_sel_hi:[1,0]\n"
      "v_pk_add_f32 v[84:85], v[84:85], 1.0 op_sel_hi:[1,0]\n"
      "v_pk_add_f32 v[86:87], v[86:87], 1.0 op_sel_hi:[1,0]\n"
      "v_pk_add_f32 v[88:89], v[88:89], 1.0 op_sel_hi:[1,0]\n"
      "v_pk_add_f32 v[90:91], v[90:91], 1.0 op_sel_hi:[1,0]\n"
      "v_pk_add_f32 v[92:93], v[92:93], 1.0 op_sel_hi:[1,0]\n"
      "v_pk_add_f32 v[94:95], v[94:95], 1.0 op_sel_hi:[1,0]\n"
      "v_mfma_f32_32x32x16_bf16 v[0:15], v[184:187], v[152:155], v[0:15]\n"
      "v_rcp_f32 v64, v64\n"
      "v_rcp_f32 v65, v65\n"
      "v_rcp_f32 v66, v66\n"
      "v_mfma_f32_32x32x16_bf16 v[16:31], v[184:187], v[168:171], v[16:31]\n"
      "v_rcp_f32 v67, v67\n"
      "v_rcp_f32 v68, v68\n"
      "v_rcp_f32 v69, v69\n"
      "v_mfma_f32_32x32x16_bf16 v[0:15], v[188:191], v[156:159], v[0:15]\n"
      "v_rcp_f32 v70, v70\n"
      "v_rcp_f32 v71, v71\n"
      "v_rcp_f32 v72, v72\n"
      "v_mfma_f32_32x32x16_bf16 v[16:31], v[188:191], v[172:175], v[16:31]\n"
      "v_rcp_f32 v73, v73\n"
      "v_rcp_f32 v74, v74\n"
      "v_rcp_f32 v75, v75\n"
      "s_waitcnt lgkmcnt(2)\n"
      "v_mfma_f32_32x32x16_bf16 v[0:15], v[200:203], v[192:195], v[0:15]\n"
      "v_rcp_f32 v76, v76\n"
      "v_rcp_f32 v77, v77\n"
      "v_rcp_f32 v78, v78\n"
      "s_waitcnt lgkmcnt(0)\n"
      "v_mfma_f32_32x32x16_bf16 v[16:31], v[200:203], v[196:199], v[16:31]\n"
      "v_rcp_f32 v79, v79\n"
      "v_rcp_f32 v80, v80\n"
      "v_rcp_f32 v81, v81\n"
      "s_cmp_lg_u32 s81, 0\n"
      "s_cbranch_scc1 LR2O_%=\n"
      "LB2O_%=:\n"
      "v_subrev_u32 v208, s75, v206\n"
      "v_min_u32 v208, 8, v208\n"
      "v_mad_u32_u24 v208, v208, v235, v236\n"
      "ds_write_b64 v208, v[204:205]\n"
      "ds_read_b64_tr_b16 v[192:193], v237 offset:0\n"
      "ds_read_b64_tr_b16 v[194:195], v237 offset:576\n"
      "ds_read_b64_tr_b16 v[196:197], v237 offset:1152\n"
      "ds_read_b64_tr_b16 v[198:199], v237 offset:1728\n"
      "ds_write_b64 v208, v[220:221]\n"
      "v_rcp_f32 v82, v82\n"
      "v_rcp_f32 v83, v83\n"
      "v_rcp_f32 v84, v84\n"
      "v_rcp_f32 v85, v85\n"
      "v_rcp_f32 v86, v86\n"
      "v_rcp_f32 v87, v87\n"
      "v_rcp_f32 v88, v88\n"
      "v_rcp_f32 v89, v89\n"
      "v_rcp_f32 v90, v90\n"
      "v_rcp_f32 v91, v91\n"
      "v_rcp_f32 v92, v92\n"
      "v_rcp_f32 v93, v93\n"
      "v_rcp_f32 v94, v94\n"
      "v_rcp_f32 v95, v95\n"
      "v_pk_mul_f32 v[64:65], v[64:65], v[32:33]\n"
      "v_pk_mul_f32 v[66:67], v[66:67], v[34:35]\n"
      "v_pk_mul_f32 v[68:69], v[68:69], v[36:37]\n"
      "v_pk_mul_f32 v[70:71], v[70:71], v[38:39]\n"
      "v_pk_mul_f32 v[72:73], v[72:73], v[40:41]\n"
      "v_pk_mul_f32 v[74:75], v[74:75], v[42:43]\n"
      "v_pk_mul_f32 v[76:77], v[76:77], v[44:45]\n"
      "v_pk_mul_f32 v[78:79], v[78:79], v[46:47]\n"
      "v_pk_mul_f32 v[80:81], v[80:81], v[48:49]\n"
      "v_pk_mul_f32 v[82:83], v[82:83], v[50:51]\n"
      "v_pk_mul_f32 v[84:85], v[84:85], v[52:53]\n"
      "v_pk_mul_f32 v[86:87], v[86:87], v[54:55]\n"
      "v_pk_mul_f32 v[88:89], v[88:89], v[56:57]\n"
      "v_pk_mul_f32 v[90:91], v[90:91], v[58:59]\n"
      "v_pk_mul_f32 v[92:93], v[92:93], v[60:61]\n"
      "v_pk_mul_f32 v[94:95], v[94:95], v[62:63]\n"
      "v_cvt_pk_bf16_f32 v128, v64, v65\n"
      "v_cvt_pk_bf16_f32 v129, v66, v67\n"
      "v_cvt_pk_bf16_f32 v130, v68, v69\n"
      "v_cvt_pk_bf16_f32 v131, v70, v71\n"
      "v_cvt_pk_bf16_f32 v132, v72, v73\n"
      "v_cvt_pk_bf16_f32 v133, v74, v75\n"
      "v_cvt_pk_bf16_f32 v134, v76, v77\n"
      "v_cvt_pk_bf16_f32 v135, v78, v79\n"
      "v_cvt_pk_bf16_f32 v136, v80, v81\n"
      "v_cvt_pk_bf16_f32 v137, v82, v83\n"
      "v_cvt_pk_bf16_f32 v138, v84, v85\n"
      "v_cvt_pk_bf16_f32 v139, v86, v87\n"
      "v_cvt_pk_bf16_f32 v140, v88, v89\n"
      "v_cvt_pk_bf16_f32 v141, v90, v91\n"
      "v_cvt_pk_bf16_f32 v142, v92, v93\n"
      "v_cvt_pk_bf16_f32 v143, v94, v95\n"
      "s_cmp_le_u32 s72, s58\n"
      "s_cbranch_scc0 LSP_%=\n"
      "s_setprio 0\n"
      "LSP_%=:\n"
      "s_sub_u32 s72, s72, 1\n"
      "s_cmp_eq_u32 s72, 0\n"
      "s_cbranch_scc0 LIE_%=\n"
      "v_mov_b32 v200, v204\n"
      "v_mov_b32 v201, v205\n"
      "v_mov_b32 v202, v206\n"
      "LTE_%=:\n"
      "v_mbcnt_lo_u32_b32 v176, -1, 0\n"
      "v_mbcnt_hi_u32_b32 v176, -1, v176\n"
      "s_mov_b32 s24, %[olo]\n"
      "s_and_b32 s25, %[ohi], 0xffff\n"
      "s_mov_b32 s26, 6700000\n"
      "s_mov_b32 s27, 0x20000\n"
      "s_mul_i32 s95, %[b], 196\n"
      "s_mov_b32 s20, 0xbf317218\n"
      "s_cmp_eq_u32 %[w], 7\n"
      "s_cbranch_scc1 LTS_%=\n"
      "s_lshl_b32 s22, %[w], 5\n"
      "s_add_u32 s22, s22, s95\n"
      "s_lshl_b32 s22, s22, 7\n"
      "v_and_b32 v177, 31, v176\n"
      "v_lshrrev_b32 v178, 5, v176\n"
      "v_lshlrev_b32 v179, 4, v176\n"
      "v_lshlrev_b32 v180, 7, v177\n"
      "v_lshl_add_u32 v180, v178, 4, v180\n"
      "buffer_load_dwordx4 v[144:147], v180, s[64:67], s22 offen offset:0\n"
      "buffer_load_dwordx4 v[148:151], v180, s[64:67], s22 offen offset:32\n"
      "buffer_load_dwordx4 v[152:155], v180, s[64:67], s22 offen offset:64\n"
      "buffer_load_dwordx4 v[156:159], v180, s[64:67], s22 offen offset:96\n"
      "s_mov_b32 s83, 7478528\n"
      "buffer_load_dwordx4 v[32:35], v179, s[52:55], s83 offen offset:0\n"
      "buffer_load_dwordx4 v[36:39], v179, s[52:55], s83 offen offset:1024\n"
      "buffer_load_dwordx4 v[40:43], v179, s[52:55], s83 offen offset:2048\n"
      "buffer_load_dwordx4 v[44:47], v179, s[52:55], s83 offen offset:3072\n"
      "s_mov_b32 s83, 7486720\n"
      "buffer_load_dwordx4 v[48:51], v179, s[52:55], s83 offen offset:0\n"
      "buffer_load_dwordx4 v[52:55], v179, s[52:55], s83 offen offset:1024\n"
      "buffer_load_dwordx4 v[56:59], v179, s[52:55], s83 offen offset:2048\n"
      "buffer_load_dwordx4 v[60:63], v179, s[52:55], s83 offen offset:3072\n"
      "s_mov_b32 s83, 7482624\n"
      "buffer_load_dwordx4 v[64:67], v179, s[52:55], s83 offen offset:0\n"
      "buffer_load_dwordx4 v[68:71], v179, s[52:55], s83 offen offset:1024\n"
      "buffer_load_dwordx4 v[72:75], v179, s[52:55], s83 offen offset:2048\n"
      "buffer_load_dwordx4 v[76:79], v179, s[52:55], s83 offen offset:3072\n"
      "s_mov_b32 s83, 7490816\n"
      "buffer_load_dwordx4 v[80:83], v179, s[52:55], s83 offen offset:0\n"
      "buffer_load_dwordx4 v[84:87], v179, s[52:55], s83 offen offset:1024\n"
      "buffer_load_dwordx4 v[88:91], v179, s[52:55], s83 offen offset:2048\n"
      "buffer_load_dwordx4 v[92:95], v179, s[52:55], s83 offen offset:3072\n"
      "LTS_%=:\n"
      "s_waitcnt lgkmcnt(3)\n"
      "v_mfma_f32_32x32x16_bf16 v[96:111], v[128:131], v[192:195], v[96:111]\n"
      "v_mfma_f32_32x32x16_bf16 v[112:127], v[136:139], v[192:195], v[112:127]\n"
      "s_waitcnt lgkmcnt(1)\n"
      "v_mfma_f32_32x32x16_bf16 v[96:111], v[132:135], v[196:199], v[96:111]\n"
      "v_mfma_f32_32x32x16_bf16 v[112:127], v[140:143], v[196:199], v[112:127]\n"
      "LFT_%=:\n"
      "v_lshl_add_u32 v192, v235, 1, s94\n"
      "ds_read_b128 v[16:19], v192 offset:0\n"
      "ds_read_b128 v[20:23], v192 offset:32\n"
      "ds_read_b128 v[24:27], v192 offset:64\n"
      "ds_read_b128 v[28:31], v192 offset:96\n"
      "s_sub_u32 s83, s89, s75\n"
      "s_min_u32 s83, s83, 8\n"
      "s_lshl_b32 s83, s83, 2\n"
      "s_bfm_b64 s[90:91], s83, 0\n"
      "s_lshl_b32 s84, s75, 7\n"
      "v_add_u32 v194, s84, v239\n"
      "s_lshl_b32 s84, s75, 4\n"
      "v_add_u32 v195, s84, v240\n"
      "v_cvt_pk_bf16_f32 v0, v96, v97\n"
      "v_cvt_pk_bf16_f32 v1, v98, v99\n"
      "v_cvt_pk_bf16_f32 v2, v100, v101\n"
      "v_cvt_pk_bf16_f32 v3, v102, v103\n"
      "v_cvt_pk_bf16_f32 v4, v104, v105\n"
      "v_cvt_pk_bf16_f32 v5, v106, v107\n"
      "v_cvt_pk_bf16_f32 v6, v108, v109\n"
      "v_cvt_pk_bf16_f32 v7, v110, v111\n"
      "v_cvt_pk_bf16_f32 v8, v112, v113\n"
      "v_cvt_pk_bf16_f32 v9, v114, v115\n"
      "v_cvt_pk_bf16_f32 v10, v116, v117\n"
      "v_cvt_pk_bf16_f32 v11, v118, v119\n"
      "v_cvt_pk_bf16_f32 v12, v120, v121\n"
      "v_cvt_pk_bf16_f32 v13, v122, v123\n"
      "v_cvt_pk_bf16_f32 v14, v124, v125\n"
      "v_cvt_pk_bf16_f32 v15, v126, v127\n"
      "s_and_b32 exec_lo, s90, 0x11111111\n"
      "s_and_b32 exec_hi, s90, 0x11111111\n"
      "ds_write_b128 v194, v[0:3] offset:0\n"
      "ds_write_b128 v194, v[4:7] offset:16\n"
      "ds_write_b128 v194, v[8:11] offset:64\n"
      "ds_write_b128 v194, v[12:15] offset:80\n"
      "s_mov_b64 exec, -1\n"
      "s_waitcnt lgkmcnt(7)\n"
      "v_mfma_f32_32x32x16_bf16 v[96:111], v[16:19], v[0:3], 0\n"
      "s_waitcnt lgkmcnt(6)\n"
      "v_mfma_f32_32x32x16_bf16 v[96:111], v[20:23], v[4:7], v[96:111]\n"
      "s_waitcnt lgkmcnt(5)\n"
      "v_mfma_f32_32x32x16_bf16 v[96:111], v[24:27], v[8:11], v[96:111]\n"
      "s_waitcnt lgkmcnt(4)\n"
      "v_mfma_f32_32x32x16_bf16 v[96:111], v[28:31], v[12:15], v[96:111]\n"
      "s_add_u32 s75, s75, 8\n"
      "s_add_u32 s76, s76, 1\n"
      "v_readlane_b32 s74, v217, s76\n"
      "v_mov_b32 v16, v195\n"
      "v_subrev_u32 v17, s75, v202\n"
      "v_min_u32 v17, 8, v17\n"
      "v_mad_u32_u24 v17, v17, v235, v236\n"
      "ds_write_b64 v17, v[200:201]\n"
      "ds_read_b64_tr_b16 v[192:193], v237 offset:0\n"
      "ds_read_b64_tr_b16 v[194:195], v237 offset:576\n"
      "ds_read_b64_tr_b16 v[196:197], v237 offset:1152\n"
      "ds_read_b64_tr_b16 v[198:199], v237 offset:1728\n"
      "ds_write_b64 v17, v[220:221]\n"
      "s_and_b32 exec_lo, s90, 0xeeeeeeee\n"
      "s_mov_b32 exec_hi, 0\n"
      "ds_write_b32 v16, v96\n"
      "s_mov_b64 exec, -1\n"
      "s_waitcnt lgkmcnt(4)\n"
      "v_mfma_f32_32x32x16_bf16 v[112:127], v[136:139], v[192:195], 0\n"
      "s_waitcnt lgkmcnt(2)\n"
      "v_mfma_f32_32x32x16_bf16 v[112:127], v[140:143], v[196:199], v[112:127]\n"
      "v_mfma_f32_32x32x16_bf16 v[96:111], v[128:131], v[192:195], 0\n"
      "v_mfma_f32_32x32x16_bf16 v[96:111], v[132:135], v[196:199], v[96:111]\n"
      "s_cmp_lt_u32 s75, s89\n"
      "s_cbranch_scc1 LFT_%=\n"
      "s_setprio 0\n"
      "s_mov_b32 m0, s82\n"
      "s_mov_b32 s60, 0\n"
      "s_cmp_eq_u32 %[w], 7\n"
      "s_cbranch_scc1 LT7_%=\n"
      "s_waitcnt vmcnt(19)\n"
      "v_lshlrev_b32 v200, 16, v144\n"
      "v_and_b32 v201, 0xffff0000, v144\n"
      "v_lshlrev_b32 v202, 16, v145\n"
      "v_and_b32 v203, 0xffff0000, v145\n"
      "v_lshlrev_b32 v204, 16, v146\n"
      "v_and_b32 v205, 0xffff0000, v146\n"
      "v_lshlrev_b32 v206, 16, v147\n"
      "v_and_b32 v207, 0xffff0000, v147\n"
      "s_waitcnt vmcnt(18)\n"
      "v_lshlrev_b32 v208, 16, v148\n"
      "v_and_b32 v209, 0xffff0000, v148\n"
      "v_lshlrev_b32 v210, 16, v149\n"
      "v_and_b32 v211, 0xffff0000, v149\n"
      "v_lshlrev_b32 v212, 16, v150\n"
      "v_and_b32 v213, 0xffff0000, v150\n"
      "v_lshlrev_b32 v214, 16, v151\n"
      "v_and_b32 v215, 0xffff0000, v151\n"
      "s_waitcnt vmcnt(17)\n"
      "v_lshlrev_b32 v216, 16, v152\n"
      "v_and_b32 v217, 0xffff0000, v152\n"
      "v_lshlrev_b32 v218, 16, v153\n"
      "v_and_b32 v219, 0xffff0000, v153\n"
      "v_lshlrev_b32 v220, 16, v154\n"
      "v_and_b32 v221, 0xffff0000, v154\n"
      "v_lshlrev_b32 v222, 16, v155\n"
      "v_and_b32 v223, 0xffff0000, v155\n"
      "s_waitcnt vmcnt(16)\n"
      "v_lshlrev_b32 v224, 16, v156\n"
      "v_and_b32 v225, 0xffff0000, v156\n"
      "v_lshlrev_b32 v226, 16, v157\n"
      "v_and_b32 v227, 0xffff0000, v157\n"
      "v_lshlrev_b32 v228, 16, v158\n"
      "v_and_b32 v229, 0xffff0000, v158\n"
      "v_lshlrev_b32 v230, 16, v159\n"
      "v_and_b32 v231, 0xffff0000, v159\n"
      "s_waitcnt vmcnt(15)\n"
      "v_mfma_f32_32x32x16_bf16 v[0:15], v[32:35], v[144:147], 0\n"
      "s_waitcnt vmcnt(11)\n"
      "v_mfma_f32_32x32x16_bf16 v[16:31], v[48:51], v[144:147], 0\n"
      "v_mfma_f32_32x32x16_bf16 v[0:15], v[36:39], v[148:151], v[0:15]\n"
      "s_waitcnt vmcnt(10)\n"
      "v_mfma_f32_32x32x16_bf16 v[16:31], v[52:55], v[148:151], v[16:31]\n"
      "v_mfma_f32_32x32x16_bf16 v[0:15], v[40:43], v[152:155], v[0:15]\n"
      "s_waitcnt vmcnt(9)\n"
      "v_mfma_f32_32x32x16_bf16 v[16:31], v[56:59], v[152:155], v[16:31]\n"
      "v_mfma_f32_32x32x16_bf16 v[0:15], v[44:47], v[156:159], v[0:15]\n"
      "s_waitcnt vmcnt(8)\n"
      "v_mfma_f32_32x32x16_bf16 v[16:31], v[60:63], v[156:159], v[16:31]\n"
      "s_lshl_b32 s83, %[w], 12\n"
      "s_add_u32 s83, s83, 61312\n"
      "v_add_u32 v181, s83, v180\n"
      "s_lshl_b32 s83, %[w], 5\n"
      "s_sub_i32 s83, %[rb], s83\n"
      "s_min_i32 s83, s83, 32\n"
      "s_max_i32 s83, s83, 0\n"
      "s_bfm_b64 s[28:29], s83, 0\n"
      "s_waitcnt vmcnt(0) lgkmcnt(0)\n"
      "s_barrier\n"
      "s_cmp_eq_u32 s83, 0\n"
      "s_cbranch_scc1 LEND_%=\n"
      "ds_read_b128 v[128:131], v181 offset:0\n"
      "ds_read_b128 v[132:135], v181 offset:32\n"
      "ds_read_b128 v[136:139], v181 offset:64\n"
      "ds_read_b128 v[140:143], v181 offset:96\n"
      "s_waitcnt lgkmcnt(3)\n"
      "v_mfma_f32_32x32x16_bf16 v[0:15], v[64:67], v[128:131], v[0:15]\n"
      "s_waitcnt lgkmcnt(2)\n"
      "v_mfma_f32_32x32x16_bf16 v[0:15], v[68:71], v[132:135], v[0:15]\n"
      "s_waitcnt lgkmcnt(1)\n"
      "v_mfma_f32_32x32x16_bf16 v[0:15], v[72:75], v[136:139], v[0:15]\n"
      "s_waitcnt lgkmcnt(0)\n"
      "v_mfma_f32_32x32x16_bf16 v[0:15], v[76:79], v[140:143], v[0:15]\n"
      "v_mfma_f32_32x32x16_bf16 v[16:31], v[80:83], v[128:131], v[16:31]\n"
      "v_mfma_f32_32x32x16_bf16 v[16:31], v[84:87], v[132:135], v[16:31]\n"
      "v_mfma_f32_32x32x16_bf16 v[16:31], v[88:91], v[136:139], v[16:31]\n"
      "v_mfma_f32_32x32x16_bf16 v[16:31], v[92:95], v[140:143], v[16:31]\n"
      "v_and_b32 v182, 7, v177\n"
      "v_xor_b32 v182, v182, v178\n"
      "v_lshlrev_b32 v182, 4, v182\n"
      "v_lshl_add_u32 v182, v177, 7, v182\n"
      "v_add_u32 v182, s79, v182\n"
      "v_xor_b32 v183, 32, v182\n"
      "v_xor_b32 v184, 64, v182\n"
      "v_xor_b32 v185, 0x60, v182\n"
      "v_lshrrev_b32 v187, 3, v176\n"
      "v_xor_b32 v186, v187, v176\n"
      "v_and_b32 v186, 7, v186\n"
      "v_lshlrev_b32 v186, 4, v186\n"
      "v_lshl_add_u32 v186, v187, 7, v186\n"
      "v_add_u32 v186, s79, v186\n"
      "v_add_u32 v187, s22, v179\n"
      "s_mul_i32 s26, %[b], 25088\n"
      "s_lshl_b32 s84, %[rb], 7\n"
      "s_add_u32 s26, s26, s84\n"
      "v_exp_f32 v32, v0\n"
      "v_exp_f32 v33, v1\n"
      "v_exp_f32 v34, v2\n"
      "v_exp_f32 v35, v3\n"
      "v_exp_f32 v36, v4\n"
      "v_exp_f32 v37, v5\n"
      "v_exp_f32 v38, v6\n"
      "v_exp_f32 v39, v7\n"
      "v_exp_f32 v40, v8\n"
      "v_exp_f32 v41, v9\n"
      "v_exp_f32 v42, v10\n"
      "v_exp_f32 v43, v11\n"
      "v_exp_f32 v44, v12\n"
      "v_exp_f32 v45, v13\n"
      "v_exp_f32 v46, v14\n"
      "v_exp_f32 v47, v15\n"
      "v_pk_add_f32 v[32:33], v[32:33], 1.0 op_sel_hi:[1,0]\n"
      "v_pk_add_f32 v[34:35], v[34:35], 1.0 op_sel_hi:[1,0]\n"
      "v_pk_add_f32 v[36:37], v[36:37], 1.0 op_sel_hi:[1,0]\n"
      "v_pk_add_f32 v[38:39], v[38:39], 1.0 op_sel_hi:[1,0]\n"
      "v_pk_add_f32 v[40:41], v[40:41], 1.0 op_sel_hi:[1,0]\n"
      "v_pk_add_f32 v[42:43], v[42:43], 1.0 op_sel_hi:[1,0]\n"
      "v_pk_add_f32 v[44:45], v[44:45], 1.0 op_sel_hi:[1,0]\n"
      "v_pk_add_f32 v[46:47], v[46:47], 1.0 op_sel_hi:[1,0]\n"
      "v_rcp_f32 v32, v32\n"
      "v_rcp_f32 v33, v33\n"
      "v_rcp_f32 v34, v34\n"
      "v_rcp_f32 v35, v35\n"
      "v_rcp_f32 v36, v36\n"
      "v_rcp_f32 v37, v37\n"
      "v_rcp_f32 v38, v38\n"
      "v_rcp_f32 v39, v39\n"
      "v_rcp_f32 v40, v40\n"
      "v_rcp_f32 v41, v41\n"
      "v_rcp_f32 v42, v42\n"
      "v_rcp_f32 v43, v43\n"
      "v_rcp_f32 v44, v44\n"
      "v_rcp_f32 v45, v45\n"
      "v_rcp_f32 v46, v46\n"
      "v_rcp_f32 v47, v47\n"
      "v_pk_mul_f32 v[32:33], v[32:33], v[0:1]\n"
      "v_pk_mul_f32 v[34:35], v[34:35], v[2:3]\n"
      "v_pk_mul_f32 v[36:37], v[36:37], v[4:5]\n"
      "v_pk_mul_f32 v[38:39], v[38:39], v[6:7]\n"
      "v_pk_mul_f32 v[40:41], v[40:41], v[8:9]\n"
      "v_pk_mul_f32 v[42:43], v[42:43], v[10:11]\n"
      "v_pk_mul_f32 v[44:45], v[44:45], v[12:13]\n"
      "v_pk_mul_f32 v[46:47], v[46:47], v[14:15]\n"
      "v_pk_fma_f32 v[32:33], v[32:33], s[20:21], v[200:201] op_sel_hi:[1,0,1]\n"
      "v_pk_fma_f32 v[34:35], v[34:35], s[20:21], v[202:203] op_sel_hi:[1,0,1]\n"
      "v_pk_fma_f32 v[36:37], v[36:37], s[20:21], v[204:205] op_sel_hi:[1,0,1]\n"
      "v_pk_fma_f32 v[38:39], v[38:39], s[20:21], v[206:207] op_sel_hi:[1,0,1]\n"
      "v_pk_fma_f32 v[40:41], v[40:41], s[20:21], v[208:209] op_sel_hi:[1,0,1]\n"
      "v_pk_fma_f32 v[42:43], v[42:43], s[20:21], v[210:211] op_sel_hi:[1,0,1]\n"
      "v_pk_fma_f32 v[44:45], v[44:45], s[20:21], v[212:213] op_sel_hi:[1,0,1]\n"
      "v_pk_fma_f32 v[46:47], v[46:47], s[20:21], v[214:215] op_sel_hi:[1,0,1]\n"
      "v_cvt_pk_bf16_f32 v96, v32, v33\n"
      "v_cvt_pk_bf16_f32 v97, v34, v35\n"
      "v_cvt_pk_bf16_f32 v98, v36, v37\n"
      "v_cvt_pk_bf16_f32 v99, v38, v39\n"
      "v_cvt_pk_bf16_f32 v100, v40, v41\n"
      "v_cvt_pk_bf16_f32 v101, v42, v43\n"
      "v_cvt_pk_bf16_f32 v102, v44, v45\n"
      "v_cvt_pk_bf16_f32 v103, v46, v47\n"
      "ds_write_b128 v182, v[96:99]\n"
      "ds_write_b128 v183, v[100:103]\n"
      "v_exp_f32 v48, v16\n"
      "v_exp_f32 v49, v17\n"
      "v_exp_f32 v50, v18\n"
      "v_exp_f32 v51, v19\n"
      "v_exp_f32 v52, v20\n"
      "v_exp_f32 v53, v21\n"
      "v_exp_f32 v54, v22\n"
      "v_exp_f32 v55, v23\n"
      "v_exp_f32 v56, v24\n"
      "v_exp_f32 v57, v25\n"
      "v_exp_f32 v58, v26\n"
      "v_exp_f32 v59, v27\n"
      "v_exp_f32 v60, v28\n"
      "v_exp_f32 v61, v29\n"
      "v_exp_f32 v62, v30\n"
      "v_exp_f32 v63, v31\n"
      "v_pk_add_f32 v[48:49], v[48:49], 1.0 op_sel_hi:[1,0]\n"
      "v_pk_add_f32 v[50:51], v[50:51], 1.0 op_sel_hi:[1,0]\n"
      "v_pk_add_f32 v[52:53], v[52:53], 1.0 op_sel_hi:[1,0]\n"
      "v_pk_add_f32 v[54:55], v[54:55], 1.0 op_sel_hi:[1,0]\n"
      "v_pk_add_f32 v[56:57], v[56:57], 1.0 op_sel_hi:[1,0]\n"
      "v_pk_add_f32 v[58:59], v[58:59], 1.0 op_sel_hi:[1,0]\n"
      "v_pk_add_f32 v[60:61], v[60:61], 1.0 op_sel_hi:[1,0]\n"
      "v_pk_add_f32 v[62:63], v[62:63], 1.0 op_sel_hi:[1,0]\n"
      "v_rcp_f32 v48, v48\n"
      "v_rcp_f32 v49, v49\n"
      "v_rcp_f32 v50, v50\n"
      "v_rcp_f32 v51, v51\n"
      "v_rcp_f32 v52, v52\n"
      "v_rcp_f32 v53, v53\n"
      "v_rcp_f32 v54, v54\n"
      "v_rcp_f32 v55, v55\n"
      "v_rcp_f32 v56, v56\n"
      "v_rcp_f32 v57, v57\n"
      "v_rcp_f32 v58, v58\n"
      "v_rcp_f32 v59, v59\n"
      "v_rcp_f32 v60, v60\n"
      "v_rcp_f32 v61, v61\n"
      "v_rcp_f32 v62, v62\n"
      "v_rcp_f32 v63, v63\n"
      "v_pk_mul_f32 v[48:49], v[48:49], v[16:17]\n"
      "v_pk_mul_f32 v[50:51], v[50:51], v[18:19]\n"
      "v_pk_mul_f32 v[52:53], v[52:53], v[20:21]\n"
      "v_pk_mul_f32 v[54:55], v[54:55], v[22:23]\n"
      "v_pk_mul_f32 v[56:57], v[56:57], v[24:25]\n"
      "v_pk_mul_f32 v[58:59], v[58:59], v[26:27]\n"
      "v_pk_mul_f32 v[60:61], v[60:61], v[28:29]\n"
      "v_pk_mul_f32 v[62:63], v[62:63], v[30:31]\n"
      "v_pk_fma_f32 v[48:49], v[48:49], s[20:21], v[216:217] op_sel_hi:[1,0,1]\n"
      "v_pk_fma_f32 v[50:51], v[50:51], s[20:21], v[218:219] op_sel_hi:[1,0,1]\n"
      "v_pk_fma_f32 v[52:53], v[52:53], s[20:21], v[220:221] op_sel_hi:[1,0,1]\n"
      "v_pk_fma_f32 v[54:55], v[54:55], s[20:21], v[222:223] op_sel_hi:[1,0,1]\n"
      "v_pk_fma_f32 v[56:57], v[56:57], s[20:21], v[224:225] op_sel_hi:[1,0,1]\n"
      "v_pk_fma_f32 v[58:59], v[58:59], s[20:21], v[226:227] op_sel_hi:[1,0,1]\n"
      "v_pk_fma_f32 v[60:61], v[60:61], s[20:21], v[228:229] op_sel_hi:[1,0,1]\n"
      "v_pk_fma_f32 v[62:63], v[62:63], s[20:21], v[230:231] op_sel_hi:[1,0,1]\n"
      "v_cvt_pk_bf16_f32 v104, v48, v49\n"
      "v_cvt_pk_bf16_f32 v105, v50, v51\n"
      "v_cvt_pk_bf16_f32 v106, v52, v53\n"
      "v_cvt_pk_bf16_f32 v107, v54, v55\n"
      "v_cvt_pk_bf16_f32 v108, v56, v57\n"
      "v_cvt_pk_bf16_f32 v109, v58, v59\n"
      "v_cvt_pk_bf16_f32 v110, v60, v61\n"
      "v_cvt_pk_bf16_f32 v111, v62, v63\n"
      "ds_write_b128 v184, v[104:107]\n"
      "ds_write_b128 v185, v[108:111]\n"
      "ds_read_b128 v[128:131], v186 offset:0\n"
      "ds_read_b128 v[132:135], v186 offset:1024\n"
      "ds_read_b128 v[136:139], v186 offset:2048\n"
      "ds_read_b128 v[140:143], v186 offset:3072\n"
      "s_waitcnt lgkmcnt(3)\n"
      "buffer_store_dwordx4 v[128:131], v187, s[24:27], 0 offen offset:0 sc1\n"
      "s_waitcnt lgkmcnt(2)\n"
      "buffer_store_dwordx4 v[132:135], v187, s[24:27], 0 offen offset:1024 sc1\n"
      "s_waitcnt lgkmcnt(1)\n"
      "buffer_store_dwordx4 v[136:139], v187, s[24:27], 0 offen offset:2048 sc1\n"
      "s_waitcnt lgkmcnt(0)\n"
      "buffer_store_dwordx4 v[140:143], v187, s[24:27], 0 offen offset:3072 sc1\n"
      "s_mov_b64 exec, -1\n"
      "s_branch LEND_%=\n"
      "LN0_%=:\n"
      "v_mov_b32 v192, 0\n"
      "v_mov_b32 v193, 0\n"
      "v_mov_b32 v194, 0\n"
      "v_mov_b32 v195, 0\n"
      "v_mov_b32 v196, 0\n"
      "v_mov_b32 v197, 0\n"
      "v_mov_b32 v198, 0\n"
      "v_mov_b32 v199, 0\n"
      "v_mov_b32 v200, 0\n"
      "v_mov_b32 v201, 0\n"
      "v_mov_b32 v202, 0xffffff\n"
      "s_branch LTE_%=\n"
      "LFE_%=:\n"
      "v_lshl_add_u32 v192, v235, 1, s94\n"
      "ds_read_b128 v[48:51], v192 offset:0\n"
      "ds_read_b128 v[52:55], v192 offset:32\n"
      "ds_read_b128 v[56:59], v192 offset:64\n"
      "ds_read_b128 v[60:63], v192 offset:96\n"
      "s_sub_u32 s83, s89, s75\n"
      "s_min_u32 s83, s83, 8\n"
      "s_lshl_b32 s83, s83, 2\n"
      "s_bfm_b64 s[90:91], s83, 0\n"
      "s_lshl_b32 s84, s75, 7\n"
      "v_add_u32 v194, s84, v239\n"
      "s_lshl_b32 s84, s75, 4\n"
      "v_add_u32 v195, s84, v240\n"
      "v_cvt_pk_bf16_f32 v32, v96, v97\n"
      "v_cvt_pk_bf16_f32 v33, v98, v99\n"
      "v_cvt_pk_bf16_f32 v34, v100, v101\n"
      "v_cvt_pk_bf16_f32 v35, v102, v103\n"
      "v_cvt_pk_bf16_f32 v36, v104, v105\n"
      "v_cvt_pk_bf16_f32 v37, v106, v107\n"
      "v_cvt_pk_bf16_f32 v38, v108, v109\n"
      "v_cvt_pk_bf16_f32 v39, v110, v111\n"
      "v_cvt_pk_bf16_f32 v40, v112, v113\n"
      "v_cvt_pk_bf16_f32 v41, v114, v115\n"
      "v_cvt_pk_bf16_f32 v42, v116, v117\n"
      "v_cvt_pk_bf16_f32 v43, v118, v119\n"
      "v_cvt_pk_bf16_f32 v44, v120, v121\n"
      "v_cvt_pk_bf16_f32 v45, v122, v123\n"
      "v_cvt_pk_bf16_f32 v46, v124, v125\n"
      "v_cvt_pk_bf16_f32 v47, v126, v127\n"
      "s_and_b32 exec_lo, s90, 0x11111111\n"
      "s_and_b32 exec_hi, s90, 0x11111111\n"
      "ds_write_b128 v194, v[32:35] offset:0\n"
      "ds_write_b128 v194, v[36:39] offset:16\n"
      "ds_write_b128 v194, v[40:43] offset:64\n"
      "ds_write_b128 v194, v[44:47] offset:80\n"
      "s_mov_b64 exec, -1\n"
      "s_waitcnt lgkmcnt(7)\n"
      "v_mfma_f32_32x32x16_bf16 v[96:111], v[48:51], v[32:35], 0\n"
      "s_waitcnt lgkmcnt(6)\n"
      "v_mfma_f32_32x32x16_bf16 v[96:111], v[52:55], v[36:39], v[96:111]\n"
      "s_waitcnt lgkmcnt(5)\n"
      "v_mfma_f32_32x32x16_bf16 v[96:111], v[56:59], v[40:43], v[96:111]\n"
      "s_waitcnt lgkmcnt(4)\n"
      "v_mfma_f32_32x32x16_bf16 v[96:111], v[60:63], v[44:47], v[96:111]\n"
      "s_add_u32 s75, s75, 8\n"
      "s_add_u32 s76, s76, 1\n"
      "v_readlane_b32 s74, v217, s76\n"
      "v_mov_b32 v48, v195\n"
      "v_subrev_u32 v49, s75, v206\n"
      "v_min_u32 v49, 8, v49\n"
      "v_mad_u32_u24 v49, v49, v235, v236\n"
      "ds_write_b64 v49, v[204:205]\n"
      "ds_read_b64_tr_b16 v[192:193], v237 offset:0\n"
      "ds_read_b64_tr_b16 v[194:195], v237 offset:576\n"
      "ds_read_b64_tr_b16 v[196:197], v237 offset:1152\n"
      "ds_read_b64_tr_b16 v[198:199], v237 offset:1728\n"
      "ds_write_b64 v49, v[220:221]\n"
      "s_and_b32 exec_lo, s90, 0xeeeeeeee\n"
      "s_mov_b32 exec_hi, 0\n"
      "ds_write_b32 v48, v96\n"
      "s_mov_b64 exec, -1\n"
      "s_waitcnt lgkmcnt(4)\n"
      "v_mfma_f32_32x32x16_bf16 v[112:127], v[136:139], v[192:195], 0\n"
      "s_waitcnt lgkmcnt(2)\n"
      "v_mfma_f32_32x32x16_bf16 v[112:127], v[140:143], v[196:199], v[112:127]\n"
      "v_mfma_f32_32x32x16_bf16 v[96:111], v[128:131], v[192:195], 0\n"
      "v_mfma_f32_32x32x16_bf16 v[96:111], v[132:135], v[196:199], v[96:111]\n"
      "s_cmp_ge_u32 s75, s89\n"
      "s_cbranch_scc1 LBFE_%=\n"
      "s_cmp_le_i32 s74, s73\n"
      "s_cbranch_scc1 LFE_%=\n"
      "s_branch LBFE_%=\n"
      "LFO_%=:\n"
      "v_lshl_add_u32 v192, v235, 1, s94\n"
      "ds_read_b128 v[16:19], v192 offset:0\n"
      "ds_read_b128 v[20:23], v192 offset:32\n"
      "ds_read_b128 v[24:27], v192 offset:64\n"
      "ds_read_b128 v[28:31], v192 offset:96\n"
      "s_sub_u32 s83, s89, s75\n"
      "s_min_u32 s83, s83, 8\n"
      "s_lshl_b32 s83, s83, 2\n"
      "s_bfm_b64 s[90:91], s83, 0\n"
      "s_lshl_b32 s84, s75, 7\n"
      "v_add_u32 v194, s84, v239\n"
      "s_lshl_b32 s84, s75, 4\n"
      "v_add_u32 v195, s84, v240\n"
      "v_cvt_pk_bf16_f32 v0, v96, v97\n"
      "v_cvt_pk_bf16_f32 v1, v98, v99\n"
      "v_cvt_pk_bf16_f32 v2, v100, v101\n"
      "v_cvt_pk_bf16_f32 v3, v102, v103\n"
      "v_cvt_pk_bf16_f32 v4, v104, v105\n"
      "v_cvt_pk_bf16_f32 v5, v106, v107\n"
      "v_cvt_pk_bf16_f32 v6, v108, v109\n"
      "v_cvt_pk_bf16_f32 v7, v110, v111\n"
      "v_cvt_pk_bf16_f32 v8, v112, v113\n"
      "v_cvt_pk_bf16_f32 v9, v114, v115\n"
      "v_cvt_pk_bf16_f32 v10, v116, v117\n"
      "v_cvt_pk_bf16_f32 v11, v118, v119\n"
      "v_cvt_pk_bf16_f32 v12, v120, v121\n"
      "v_cvt_pk_bf16_f32 v13, v122, v123\n"
      "v_cvt_pk_bf16_f32 v14, v124, v125\n"
      "v_cvt_pk_bf16_f32 v15, v126, v127\n"
      "s_and_b32 exec_lo, s90, 0x11111111\n"
      "s_and_b32 exec_hi, s90, 0x11111111\n"
      "ds_write_b128 v194, v[0:3] offset:0\n"
      "ds_write_b128 v194, v[4:7] offset:16\n"
      "ds_write_b128 v194, v[8:11] offset:64\n"
      "ds_write_b128 v194, v[12:15] offset:80\n"
      "s_mov_b64 exec, -1\n"
      "s_waitcnt lgkmcnt(7)\n"
      "v_mfma_f32_32x32x16_bf16 v[96:111], v[16:19], v[0:3], 0\n"
      "s_waitcnt lgkmcnt(6)\n"
      "v_mfma_f32_32x32x16_bf16 v[96:111], v[20:23], v[4:7], v[96:111]\n"
      "s_waitcnt lgkmcnt(5)\n"
      "v_mfma_f32_32x32x16_bf16 v[96:111], v[24:27], v[8:11], v[96:111]\n"
      "s_waitcnt lgkmcnt(4)\n"
      "v_mfma_f32_32x32x16_bf16 v[96:111], v[28:31], v[12:15], v[96:111]\n"
      "s_add_u32 s75, s75, 8\n"
      "s_add_u32 s76, s76, 1\n"
      "v_readlane_b32 s74, v217, s76\n"
      "v_mov_b32 v16, v195\n"
      "v_subrev_u32 v17, s75, v202\n"
      "v_min_u32 v17, 8, v17\n"
      "v_mad_u32_u24 v17, v17, v235, v236\n"
      "ds_write_b64 v17, v[200:201]\n"
      "ds_read_b64_tr_b16 v[192:193], v237 offset:0\n"
      "ds_read_b64_tr_b16 v[194:195], v237 offset:576\n"
      "ds_read_b64_tr_b16 v[196:197], v237 offset:1152\n"
      "ds_read_b64_tr_b16 v[198:199], v237 offset:1728\n"
      "ds_write_b64 v17, v[220:221]\n"
      "s_and_b32 exec_lo, s90, 0xeeeeeeee\n"
      "s_mov_b32 exec_hi, 0\n"
      "ds_write_b32 v16, v96\n"
      "s_mov_b64 exec, -1\n"
      "s_waitcnt lgkmcnt(4)\n"
      "v_mfma_f32_32x32x16_bf16 v[112:127], v[136:139], v[192:195], 0\n"
      "s_waitcnt lgkmcnt(2)\n"
      "v_mfma_f32_32x32x16_bf16 v[112:127], v[140:143], v[196:199], v[112:127]\n"
      "v_mfma_f32_32x32x16_bf16 v[96:111], v[128:131], v[192:195], 0\n"
      "v_mfma_f32_32x32x16_bf16 v[96:111], v[132:135], v[196:199], v[96:111]\n"
      "s_cmp_ge_u32 s75, s89\n"
      "s_cbranch_scc1 LBFO_%=\n"
      "s_cmp_le_i32 s74, s73\n"
      "s_cbranch_scc1 LFO_%=\n"
      "s_branch LBFO_%=\n"
      "LR1P_%=:\n"
      "v_min_u32 v200, 8, v200\n"
      "s_mov_b32 s81, 1\n"
      "s_branch LB1P_%=\n"
      "LR2P_%=:\n"
      "s_mov_b32 s81, 0\n"
      "s_mov_b32 s88, s77\n"
      "v_lshlrev_b32 v188, 4, v235\n"
      "v_add_u32 v188, s87, v188\n"
      "LR2LP_%=:\n"
      "s_add_u32 s88, s88, 8\n"
      "s_lshl_b32 s83, s88, 3\n"
      "v_subrev_u32 v189, s88, v208\n"
      "v_cmp_le_i32 vcc, 8, v189\n"
      "v_min_u32 v189, 8, v189\n"
      "v_mad_u32_u24 v189, v189, v234, v188\n"
      "v_mad_u32_u24 v186, s83, v234, v233\n"
      "ds_read_b128 v[176:179], v189\n"
      "ds_read_b64_tr_b16 v[180:181], v186 offset:0\n"
      "ds_read_b64_tr_b16 v[182:183], v186 offset:512\n"
      "ds_read_b64_tr_b16 v[184:185], v186 offset:64\n"
      "ds_read_b64_tr_b16 v[186:187], v186 offset:576\n"
      "s_waitcnt lgkmcnt(2)\n"
      "v_mfma_f32_32x32x16_bf16 v[0:15], v[176:179], v[180:183], v[0:15]\n"
      "s_waitcnt lgkmcnt(0)\n"
      "v_mfma_f32_32x32x16_bf16 v[16:31], v[176:179], v[184:187], v[16:31]\n"
      "s_cbranch_vccz LB2P_%=\n"
      "s_cmp_lt_u32 s88, 200\n"
      "s_cbranch_scc1 LR2LP_%=\n"
      "s_branch LB2P_%=\n"
      "LR1E_%=:\n"
      "v_min_u32 v204, 8, v204\n"
      "s_mov_b32 s81, 1\n"
      "s_branch LB1E_%=\n"
      "LR2E_%=:\n"
      "s_mov_b32 s81, 0\n"
      "s_mov_b32 s88, s77\n"
      "v_lshlrev_b32 v188, 4, v235\n"
      "v_add_u32 v188, s87, v188\n"
      "LR2LE_%=:\n"
      "s_add_u32 s88, s88, 8\n"
      "s_lshl_b32 s83, s88, 3\n"
      "v_subrev_u32 v189, s88, v208\n"
      "v_cmp_le_i32 vcc, 8, v189\n"
      "v_min_u32 v189, 8, v189\n"
      "v_mad_u32_u24 v189, v189, v234, v188\n"
      "v_mad_u32_u24 v186, s83, v234, v233\n"
      "ds_read_b128 v[176:179], v189\n"
      "ds_read_b64_tr_b16 v[180:181], v186 offset:0\n"
      "ds_read_b64_tr_b16 v[182:183], v186 offset:512\n"
      "ds_read_b64_tr_b16 v[184:185], v186 offset:64\n"
      "ds_read_b64_tr_b16 v[186:187], v186 offset:576\n"
      "s_waitcnt lgkmcnt(2)\n"
      "v_mfma_f32_32x32x16_bf16 v[32:47], v[176:179], v[180:183], v[32:47]\n"
      "s_waitcnt lgkmcnt(0)\n"
      "v_mfma_f32_32x32x16_bf16 v[48:63], v[176:179], v[184:187], v[48:63]\n"
      "s_cbranch_vccz LB2E_%=\n"
      "s_cmp_lt_u32 s88, 200\n"
      "s_cbranch_scc1 LR2LE_%=\n"
      "s_branch LB2E_%=\n"
      "LR1O_%=:\n"
      "v_min_u32 v200, 8, v200\n"
      "s_mov_b32 s81, 1\n"
      "s_branch LB1O_%=\n"
      "LR2O_%=:\n"
      "s_mov_b32 s81, 0\n"
      "s_mov_b32 s88, s77\n"
      "v_lshlrev_b32 v188, 4, v235\n"
      "v_add_u32 v188, s87, v188\n"
      "LR2LO_%=:\n"
      "s_add_u32 s88, s88, 8\n"
      "s_lshl_b32 s83, s88, 3\n"
      "v_subrev_u32 v189, s88, v208\n"
      "v_cmp_le_i32 vcc, 8, v189\n"
      "v_min_u32 v189, 8, v189\n"
      "v_mad_u32_u24 v189, v189, v234, v188\n"
      "v_mad_u32_u24 v186, s83, v234, v233\n"
      "ds_read_b128 v[176:179], v189\n"
      "ds_read_b64_tr_b16 v[180:181], v186 offset:0\n"
      "ds_read_b64_tr_b16 v[182:183], v186 offset:512\n"
      "ds_read_b64_tr_b16 v[184:185], v186 offset:64\n"
      "ds_read_b64_tr_b16 v[186:187], v186 offset:576\n"
      "s_waitcnt lgkmcnt(2)\n"
      "v_mfma_f32_32x32x16_bf16 v[0:15], v[176:179], v[180:183], v[0:15]\n"
      "s_waitcnt lgkmcnt(0)\n"
      "v_mfma_f32_32x32x16_bf16 v[16:31], v[176:179], v[184:187], v[16:31]\n"
      "s_cbranch_vccz LB2O_%=\n"
      "s_cmp_lt_u32 s88, 200\n"
      "s_cbranch_scc1 LR2LO_%=\n"
      "s_branch LB2O_%=\n"
      "LT7_%=:\n"
      "s_mul_i32 s30, %[rb], 3\n"
      "s_lshr_b32 s30, s30, 1\n"
      "s_mul_i32 s23, s95, 6\n"
      "s_add_u32 s23, s23, 6400000\n"
      "s_waitcnt vmcnt(0)\n"
      "v_add_u32 v0, 0, v176\n"
      "v_cmp_gt_u32_e64 s[34:35], s30, v0\n"
      "v_lshlrev_b32 v8, 1, v0\n"
      "v_mul_u32_u24 v32, 21846, v8\n"
      "v_lshrrev_b32 v32, 16, v32\n"
      "v_add_lshl_u32 v8, v8, v32, 2\n"
      "ds_read_b32 v32, v8 offset:26752\n"
      "v_lshlrev_b32 v9, 1, v0\n"
      "v_or_b32 v9, 1, v9\n"
      "v_mul_u32_u24 v33, 21846, v9\n"
      "v_lshrrev_b32 v33, 16, v33\n"
      "v_add_lshl_u32 v9, v9, v33, 2\n"
      "ds_read_b32 v33, v9 offset:26752\n"
      "v_add_u32 v1, 64, v176\n"
      "v_cmp_gt_u32_e64 s[36:37], s30, v1\n"
      "v_lshlrev_b32 v10, 1, v1\n"
      "v_mul_u32_u24 v34, 21846, v10\n"
      "v_lshrrev_b32 v34, 16, v34\n"
      "v_add_lshl_u32 v10, v10, v34, 2\n"
      "ds_read_b32 v34, v10 offset:26752\n"
      "v_lshlrev_b32 v11, 1, v1\n"
      "v_or_b32 v11, 1, v11\n"
      "v_mul_u32_u24 v35, 21846, v11\n"
      "v_lshrrev_b32 v35, 16, v35\n"
      "v_add_lshl_u32 v11, v11, v35, 2\n"
      "ds_read_b32 v35, v11 offset:26752\n"
      "v_add_u32 v2, 128, v176\n"
      "v_cmp_gt_u32_e64 s[38:39], s30, v2\n"
      "v_lshlrev_b32 v12, 1, v2\n"
      "v_mul_u32_u24 v36, 21846, v12\n"
      "v_lshrrev_b32 v36, 16, v36\n"
      "v_add_lshl_u32 v12, v12, v36, 2\n"
      "ds_read_b32 v36, v12 offset:26752\n"
      "v_lshlrev_b32 v13, 1, v2\n"
      "v_or_b32 v13, 1, v13\n"
      "v_mul_u32_u24 v37, 21846, v13\n"
      "v_lshrrev_b32 v37, 16, v37\n"
      "v_add_lshl_u32 v13, v13, v37, 2\n"
      "ds_read_b32 v37, v13 offset:26752\n"
      "v_add_u32 v3, 192, v176\n"
      "v_cmp_gt_u32_e64 s[40:41], s30, v3\n"
      "v_lshlrev_b32 v14, 1, v3\n"
      "v_mul_u32_u24 v38, 21846, v14\n"
      "v_lshrrev_b32 v38, 16, v38\n"
      "v_add_lshl_u32 v14, v14, v38, 2\n"
      "ds_read_b32 v38, v14 offset:26752\n"
      "v_lshlrev_b32 v15, 1, v3\n"
      "v_or_b32 v15, 1, v15\n"
      "v_mul_u32_u24 v39, 21846, v15\n"
      "v_lshrrev_b32 v39, 16, v39\n"
      "v_add_lshl_u32 v15, v15, v39, 2\n"
      "ds_read_b32 v39, v15 offset:26752\n"
      "v_add_u32 v4, 256, v176\n"
      "v_cmp_gt_u32_e64 s[42:43], s30, v4\n"
      "v_lshlrev_b32 v16, 1, v4\n"
      "v_mul_u32_u24 v40, 21846, v16\n"
      "v_lshrrev_b32 v40, 16, v40\n"
      "v_add_lshl_u32 v16, v16, v40, 2\n"
      "ds_read_b32 v40, v16 offset:26752\n"
      "v_lshlrev_b32 v17, 1, v4\n"
      "v_or_b32 v17, 1, v17\n"
      "v_mul_u32_u24 v41, 21846, v17\n"
      "v_lshrrev_b32 v41, 16, v41\n"
      "v_add_lshl_u32 v17, v17, v41, 2\n"
      "ds_read_b32 v41, v17 offset:26752\n"
      "v_lshlrev_b32 v179, 2, v176\n"
      "s_waitcnt lgkmcnt(0)\n"
      "s_barrier\n"
      "ds_read_b32 v64, v8 offset:21764\n"
      "ds_read_b32 v65, v9 offset:21764\n"
      "ds_read_b32 v66, v10 offset:21764\n"
      "ds_read_b32 v67, v11 offset:21764\n"
      "ds_read_b32 v68, v12 offset:21764\n"
      "ds_read_b32 v69, v13 offset:21764\n"
      "ds_read_b32 v70, v14 offset:21764\n"
      "ds_read_b32 v71, v15 offset:21764\n"
      "ds_read_b32 v72, v16 offset:21764\n"
      "ds_read_b32 v73, v17 offset:21764\n"
      "s_waitcnt lgkmcnt(9)\n"
      "v_fmac_f32 v32, s20, v64\n"
      "s_waitcnt lgkmcnt(8)\n"
      "v_fmac_f32 v33, s20, v65\n"
      "v_cvt_pk_bf16_f32 v80, v32, v33\n"
      "s_waitcnt lgkmcnt(7)\n"
      "v_fmac_f32 v34, s20, v66\n"
      "s_waitcnt lgkmcnt(6)\n"
      "v_fmac_f32 v35, s20, v67\n"
      "v_cvt_pk_bf16_f32 v81, v34, v35\n"
      "s_waitcnt lgkmcnt(5)\n"
      "v_fmac_f32 v36, s20, v68\n"
      "s_waitcnt lgkmcnt(4)\n"
      "v_fmac_f32 v37, s20, v69\n"
      "v_cvt_pk_bf16_f32 v82, v36, v37\n"
      "s_waitcnt lgkmcnt(3)\n"
      "v_fmac_f32 v38, s20, v70\n"
      "s_waitcnt lgkmcnt(2)\n"
      "v_fmac_f32 v39, s20, v71\n"
      "v_cvt_pk_bf16_f32 v83, v38, v39\n"
      "s_waitcnt lgkmcnt(1)\n"
      "v_fmac_f32 v40, s20, v72\n"
      "s_waitcnt lgkmcnt(0)\n"
      "v_fmac_f32 v41, s20, v73\n"
      "v_cvt_pk_bf16_f32 v84, v40, v41\n"
      "s_mov_b64 exec, s[34:35]\n"
      "buffer_store_dword v80, v179, s[24:27], s23 offen offset:0 sc1\n"
      "s_mov_b64 exec, s[36:37]\n"
      "buffer_store_dword v81, v179, s[24:27], s23 offen offset:256 sc1\n"
      "s_mov_b64 exec, s[38:39]\n"
      "buffer_store_dword v82, v179, s[24:27], s23 offen offset:512 sc1\n"
      "s_mov_b64 exec, s[40:41]\n"
      "buffer_store_dword v83, v179, s[24:27], s23 offen offset:768 sc1\n"
      "s_mov_b64 exec, s[42:43]\n"
      "buffer_store_dword v84, v179, s[24:27], s23 offen offset:1024 sc1\n"
      "s_mov_b64 exec, -1\n"
      "s_branch LEND_%=\n"
      "LBAIL_%=:\n"
      "s_mov_b64 exec, -1\n"
      "s_waitcnt vmcnt(0) lgkmcnt(0)\n"
      "s_nop 15\n"
      "s_nop 15\n"
      "s_barrier\n"
      "s_mov_b32 m0, s82\n"
      "s_mov_b32 s60, 1\n"
      "s_branch LEND_%=\n"
      "LEND_%=:\n"
      "s_mov_b32 %[flag], s60\n"
      : [flag] "=&s"(flag)
      : [b] "s"(b), [rb] "s"(Rb), [olo] "s"((u32)op), [ohi] "s"((u32)(op >> 32)), [w] "s"(w), [hlo] "s"((u32)hp), [hhi] "s"((u32)(hp >> 32)), [wlo] "s"((u32)wp), [whi] "s"((u32)(wp >> 32))
      : "v0", "v1", "v2", "v3", "v4", "v5", "v6", "v7", "v8", "v9", "v10", "v11", "v12", "v13", "v14", "v15", "v16", "v17", "v18", "v19", "v20", "v21", "v22", "v23", "v24", "v25", "v26", "v27", "v28", "v29", "v30", "v31", "v32", "v33", "v34", "v35", "v36", "v37", "v38", "v39", "v40", "v41", "v42", "v43", "v44", "v45", "v46", "v47", "v48", "v49", "v50", "v51", "v52", "v53", "v54", "v55", "v56", "v57", "v58", "v59", "v60", "v61", "v62", "v63", "v64", "v65", "v66", "v67", "v68", "v69", "v70", "v71", "v72", "v73", "v74", "v75", "v76", "v77", "v78", "v79", "v80", "v81", "v82", "v83", "v84", "v85", "v86", "v87", "v88", "v89", "v90", "v91", "v92", "v93", "v94", "v95", "v96", "v97", "v98", "v99", "v100", "v101", "v102", "v103", "v104", "v105", "v106", "v107", "v108", "v109", "v110", "v111", "v112", "v113", "v114", "v115", "v116", "v117", "v118", "v119", "v120", "v121", "v122", "v123", "v124", "v125", "v126", "v127", "v128", "v129", "v130", "v131", "v132", "v133", "v134", "v135", "v136", "v137", "v138", "v139", "v140", "v141", "v142", "v143", "v144", "v145", "v146", "v147", "v148", "v149", "v150", "v151", "v152", "v153", "v154", "v155", "v156", "v157", "v158", "v159", "v160", "v161", "v162", "v163", "v164", "v165", "v166", "v167", "v168", "v169", "v170", "v171", "v172", "v173", "v174", "v175", "v176", "v177", "v178", "v179", "v180", "v181", "v182", "v183", "v184", "v185", "v186", "v187", "v188", "v189", "v190", "v191", "v192", "v193", "v194", "v195", "v196", "v197", "v198", "v199", "v200", "v201", "v202", "v203", "v204", "v205", "v206", "v207", "v208", "v209", "v210", "v211", "v212", "v213", "v214", "v215", "v216", "v217", "v218", "v219", "v220", "v221", "v222", "v223", "v224", "v225", "v226", "v227", "v228", "v229", "v230", "v231", "v232", "v233", "v234", "v235", "v236", "v237", "v238", "v239", "v240", "v241", "v242", "s20", "s21", "s22", "s23", "s24", "s25", "s26", "s27", "s28", "s29", "s30", "s31", "s34", "s35", "s36", "s37", "s38", "s39", "s40", "s41", "s42", "s43", "s44", "s45", "s46", "s47", "s48", "s49", "s50", "s51", "s52", "s53", "s54", "s55", "s56", "s57", "s58", "s59", "s60", "s61", "s62", "s63", "s64", "s65", "s66", "s67", "s68", "s69", "s70", "s71", "s72", "s73", "s74", "s75", "s76", "s77", "s78", "s79", "s80", "s81", "s82", "s83", "s84", "s85", "s86", "s87", "s88", "s89", "s90", "s91", "s92", "s93", "s94", "s95", "vcc", "scc", "memory");
  }
  int lane;
  asm volatile("v_mbcnt_lo_u32_b32 %0, -1, 0\n v_mbcnt_hi_u32_b32 %0, -1, %0" : "=v"(lane));
  const int tid = w * 64 + lane;
  const int r = lane & 31, hh = lane >> 5;
  if (flag) {
  const __amdgpu_buffer_rsrc_t srdH = __builtin_amdgcn_make_buffer_rsrc((void*)h, 0, NN * 128, 0x00020000);
  u32* hist = (u32*)(smem + L_HIST);
  u32* hist2 = (u32*)(smem + L_HIST2);
  u32* cur2 = (u32*)(smem + L_CUR2);
  u32* start = (u32*)(smem + L_START);
  u32* LA = (u32*)(smem + L_LA);
  unsigned char* LD = (unsigned char*)(smem + L_LD);
  const u64* __restrict__ rec = (const u64*)(ws + WS_REC);
  const f32x4* __restrict__ posf = (const f32x4*)(ws + WS_POS);

  u32 oc = 0;
  {
    const int p = 32 * w + r;
    if (p < NP) oc = ((const u32*)(ws + WS_OFF))[b * 256 + (p & 7) * 32 + (p >> 3)];
  }
  u32x4 hb[4], w1[8];
  if (w < 7) {
#pragma unroll
    for (int s = 0; s < 4; s++)
      hb[s] = __builtin_bit_cast(u32x4, __builtin_amdgcn_raw_buffer_load_b128(srdH, (n0 + 32 * w + r) * 128 + (2 * s + hh) * 16, 0, 0));
#pragma unroll
    for (int i = 0; i < 8; i++) w1[i] = ((const u32x4*)(ws + WS_W1F))[i * 64 + lane];
  }
  if (tid < 208) {
    hist[tid] = 0;
    hist2[tid] = 0;
    cur2[tid] = 0;
    f32x4 pz = {0.f, 0.f, 0.f, 0.f};
    f32x4 pv = pz;
    if (tid < Rb) pv = posf[n0 + tid];
    ((f32x4*)(smem + L_PO))[tid] = pv;
    if (tid < 200) ((f32x4*)(smem + L_PD))[tid] = pz;
  } else if (tid < 208 + 72) {
    const int q = tid - 208;
    ((u32x4*)(smem + L_CR))[q] = ((const u32x4*)(ws + WS_CR))[q];
  } else if (tid < 208 + 72 + 18) {
    const int q2 = tid - 280, q = q2 >= 9 ? q2 - 9 : q2;
    u32x4 o;
#pragma unroll
    for (int i = 0; i < 4; i++) o[i] = (q >> 1) == i ? (0x3f80u << (16 * (q & 1))) : 0u;
    ((u32x4*)(smem + L_OH + (q2 >= 9 ? 512 : 0)))[q] = o;
  }
  for (int i = tid; i < 8 * 2304 / 16; i += 512) {
    u32x4 z = {0, 0, 0, 0};
    ((u32x4*)(smem + L_SP))[i] = z;
  }
  __syncthreads();


  u32 rlo[16], rhi[16];
  u32 cmax = 0;
#pragma unroll
  for (int i = 0; i < 16; i++) {
    const u32 o = (u32)__shfl((int)oc, 2 * i + hh);
    const u32 st = o & 0xffffu, cnt = o >> 16;
    const int p = 32 * w + 2 * i + hh;
    rlo[i] = 0;
    rhi[i] = 0xffffffffu;
    if ((u32)r < cnt) {
      const u64 v = rec[p * CH + st + r];
      rlo[i] = (u32)v;
      rhi[i] = (u32)(v >> 32);
    }
  }
  cmax = oc >> 16;
  const bool extras = __ballot(cmax > 32) != 0;

  if (w < 7) {
    f32x16 a0 = {}, a1 = {};
#pragma unroll
    for (int s = 0; s < 4; s++) {
      a0 = __builtin_amdgcn_mfma_f32_32x32x16_bf16(__builtin_bit_cast(bf16x8, w1[s]), __builtin_bit_cast(bf16x8, hb[s]), a0, 0, 0, 0);
      a1 = __builtin_amdgcn_mfma_f32_32x32x16_bf16(__builtin_bit_cast(bf16x8, w1[4 + s]), __builtin_bit_cast(bf16x8, hb[s]), a1, 0, 0, 0);
    }
    const int row = 32 * w + r;
    if (row < 200) {
      u32x4* dstp = (u32x4*)(smem + L_AN + row * 128 + hh * 32);
#pragma unroll
      for (int q = 0; q < 2; q++) {
        u32x4 o0, o1;
#pragma unroll
        for (int i = 0; i < 4; i++) {
          o0[i] = pack2(-LOG2E * a0[8 * q + 2 * i], -LOG2E * a0[8 * q + 2 * i + 1]);
          o1[i] = pack2(-LOG2E * a1[8 * q + 2 * i], -LOG2E * a1[8 * q + 2 * i + 1]);
        }
        dstp[q] = o0;
        dstp[4 + q] = o1;
      }
    }
  }


#pragma unroll
  for (int i = 0; i < 16; i++) {
    if (rhi[i] != 0xffffffffu) rhi[i] = (rhi[i] >> 2) | (atomicAdd(&hist[rhi[i] >> 2], 1u) << 8);
  }
  if (extras) {
    for (int i = 0; i < 32; i++) {
      const u32 o = (u32)__shfl((int)oc, i);
      const u32 st = o & 0xffffu, cnt = o >> 16;
      const int p = 32 * w + i;
      for (u32 j = 32 + lane; j < cnt; j += 64) atomicAdd(&hist2[(u32)(rec[p * CH + st + j] >> 34)], 1u);
    }
  }
  __syncthreads();


  u32 T;
  u32x4 sb;
  {
    u32x4 c = {0, 0, 0, 0};
    if (lane < 52) {
      c = ((const u32x4*)hist)[lane];
      const u32x4 c2 = ((const u32x4*)hist2)[lane];
      c += c2;
    }
    const u32 tot = c.x + c.y + c.z + c.w;
    const u32 inc = wave_incl_scan(tot);
    const u32 ex = inc - tot;
    T = (u32)__builtin_amdgcn_readlane((int)inc, 63);
    sb = u32x4{ex, ex + c.x, ex + c.x + c.y, ex + c.x + c.y + c.z};
    if (lane < 52) ((u32x4*)start)[lane] = sb;
  }
  const bool slow = T > (u32)CAP;

  if (!slow) {
#pragma unroll
    for (int i = 0; i < 16; i++) {
      if (rhi[i] != 0xffffffffu) {
        const u32 dl = rhi[i] & 0xffu;
        const u32 q = start[dl] + (rhi[i] >> 8);
        LA[q] = rlo[i];
        LD[q] = (unsigned char)dl;
      }
    }
    if (extras) {
      for (int i = 0; i < 32; i++) {
        const u32 o = (u32)__shfl((int)oc, i);
        const u32 st = o & 0xffffu, cnt = o >> 16;
        const int p = 32 * w + i;
        for (u32 j = 32 + lane; j < cnt; j += 64) {
          const u64 v = rec[p * CH + st + j];
          const u32 dl = (u32)(v >> 34);
          const u32 q = start[dl] + hist[dl] + atomicAdd(&cur2[dl], 1u);
          LA[q] = (u32)v;
          LD[q] = (unsigned char)dl;
        }
      }
    }
    if (tid < 192) {
      LA[T + tid] = 0;
      LD[T + tid] = (unsigned char)Rb;
    }
    __syncthreads();


    const u32 tg0 = (T * (u32)w) >> 3, tg1 = (T * (u32)(w + 1)) >> 3;
    int nlo = 0, nhi = 0;
#pragma unroll
    for (int i = 0; i < 4; i++) {
      const bool in = lane < 52 && 4 * lane + i < R;
      nlo += __popcll(__ballot(in && sb[i] < tg0));
      nhi += __popcll(__ballot(in && sb[i] < tg1));
    }
    if (w == 7) nhi = Rb;
    nlo = __builtin_amdgcn_readfirstlane(nlo);
    nhi = __builtin_amdgcn_readfirstlane(nhi);
    const int e0 = __builtin_amdgcn_readfirstlane((int)start[nlo]);
    const int e1 = __builtin_amdgcn_readfirstlane((int)start[nhi]);


      {
        const uint64_t hp = (uint64_t)h, wp = (uint64_t)ws, op = (uint64_t)out; (void)op;
        asm volatile(
          "s_mov_b32 s82, m0\n"
          "s_mov_b32 s64, %[hlo]\n"
          "s_and_b32 s65, %[hhi], 0xffff\n"
          "s_mov_b32 s66, 6400000\n"
          "s_mov_b32 s67, 0x20000\n"
          "s_mov_b32 s52, %[wlo]\n"
          "s_and_b32 s53, %[whi], 0xffff\n"
          "s_mov_b32 s54, 0x1000000\n"
          "s_mov_b32 s55, 0x20000\n"
          "s_add_u32 s68, %[wlo], 6662144\n"
          "s_addc_u32 s69, %[whi], 0\n"
          "s_and_b32 s69, s69, 0xffff\n"
          "s_mov_b32 s70, 800000\n"
          "s_mov_b32 s71, 0x20000\n"
          "v_mbcnt_lo_u32_b32 v208, -1, 0\n"
          "v_mbcnt_hi_u32_b32 v208, -1, v208\n"
          "v_and_b32 v209, 31, v208\n"
          "v_lshrrev_b32 v210, 5, v208\n"
          "v_lshlrev_b32 v211, 4, v208\n"
          "s_mov_b32 s83, 7470336\n"
          "buffer_load_dwordx4 v[144:147], v211, s[52:55], s83 offen offset:0\n"
          "buffer_load_dwordx4 v[148:151], v211, s[52:55], s83 offen offset:1024\n"
          "buffer_load_dwordx4 v[152:155], v211, s[52:55], s83 offen offset:2048\n"
          "buffer_load_dwordx4 v[156:159], v211, s[52:55], s83 offen offset:3072\n"
          "s_mov_b32 s83, 7474432\n"
          "buffer_load_dwordx4 v[160:163], v211, s[52:55], s83 offen offset:0\n"
          "buffer_load_dwordx4 v[164:167], v211, s[52:55], s83 offen offset:1024\n"
          "buffer_load_dwordx4 v[168:171], v211, s[52:55], s83 offen offset:2048\n"
          "buffer_load_dwordx4 v[172:175], v211, s[52:55], s83 offen offset:3072\n"
          "s_lshl_b32 s79, %[w], 13\n"
          "s_add_u32 s79, s79, 86912\n"
          "s_mov_b32 s80, 128\n"
          "s_mov_b32 s86, 0xffff\n"
          "s_mov_b32 s81, 0\n"
          "s_mov_b32 s85, 26752\n"
          "s_mov_b32 s87, 32000\n"
          "s_mov_b32 s94, 31872\n"
          "s_mov_b32 s76, 0\n"
          "s_mov_b32 s61, %[e0]\n"
          "s_mov_b32 s62, %[e1]\n"
          "s_mov_b32 s63, %[nlo]\n"
          "s_mov_b32 s89, %[nhi]\n"
          "v_and_b32 v64, 7, v208\n"
          "v_lshrrev_b32 v65, 4, v208\n"
          "v_xor_b32 v64, v64, v65\n"
          "v_lshlrev_b32 v226, 4, v64\n"
          "v_xor_b32 v227, 64, v226\n"
          "v_lshrrev_b32 v64, 1, v209\n"
          "v_and_b32 v64, 7, v64\n"
          "v_xor_b32 v64, v64, v210\n"
          "v_lshlrev_b32 v64, 4, v64\n"
          "v_lshl_add_u32 v64, v209, 7, v64\n"
          "v_add_u32 v222, s79, v64\n"
          "v_xor_b32 v223, 32, v222\n"
          "v_xor_b32 v224, 64, v222\n"
          "v_xor_b32 v225, 0x60, v222\n"
          "s_lshl_b32 s83, %[w], 10\n"
          "s_add_u32 s83, s83, 33024\n"
          "v_lshl_add_u32 v231, v209, 4, s83\n"
          "v_lshl_add_u32 v238, v208, 4, s83\n"
          "v_lshlrev_b32 v235, 3, v210\n"
          "v_lshlrev_b32 v64, 4, v210\n"
          "v_sub_u32 v234, 16, v64\n"
          "v_cmp_eq_u32 vcc, 0, v210\n"
          "v_mov_b32 v64, s87\n"
          "s_nop 0\n"
          "v_cndmask_b32 v232, v231, v64, vcc\n"
          "v_lshrrev_b32 v64, 2, v208\n"
          "v_and_b32 v64, 3, v64\n"
          "v_and_b32 v65, 3, v208\n"
          "v_lshlrev_b32 v65, 3, v65\n"
          "v_and_b32 v66, 16, v208\n"
          "v_lshl_add_u32 v65, v66, 1, v65\n"
          "v_lshl_add_u32 v66, v64, 7, v65\n"
          "v_mov_b32 v233, 30848\n"
          "v_mov_b32 v220, 61312\n"
          "v_cndmask_b32 v233, v233, v220, vcc\n"
          "v_add_u32 v233, v233, v66\n"
          "s_mul_i32 s83, %[w], 2304\n"
          "s_add_u32 s83, s83, 41216\n"
          "v_lshl_add_u32 v64, v210, 2, v64\n"
          "v_mul_u32_u24 v64, 72, v64\n"
          "v_add_u32 v64, v64, v65\n"
          "v_add_u32 v237, s83, v64\n"
          "v_mul_u32_u24 v64, 72, v209\n"
          "v_add_u32 v64, s83, v64\n"
          "v_lshlrev_b32 v65, 6, v210\n"
          "v_sub_u32 v65, 64, v65\n"
          "v_add_u32 v236, v64, v65\n"
          "v_lshrrev_b32 v64, 2, v209\n"
          "v_lshlrev_b32 v64, 7, v64\n"
          "v_lshl_add_u32 v64, v210, 5, v64\n"
          "v_add_u32 v239, 61312, v64\n"
          "v_lshlrev_b32 v64, 2, v208\n"
          "v_add_u32 v240, 21760, v64\n"
          "v_mov_b32 v220, 0\n"
          "v_mov_b32 v221, 0\n"
          "v_lshlrev_b32 v64, 3, v208\n"
          "s_add_u32 s83, s63, 8\n"
          "v_add_u32 v64, s83, v64\n"
          "v_min_u32 v64, s89, v64\n"
          "v_lshlrev_b32 v64, 2, v64\n"
          "ds_read_b32 v217, v64 offset:25856\n"
          "v_mov_b32 v96, 0\n"
          "v_mov_b32 v97, 0\n"
          "v_mov_b32 v98, 0\n"
          "v_mov_b32 v99, 0\n"
          "v_mov_b32 v100, 0\n"
          "v_mov_b32 v101, 0\n"
          "v_mov_b32 v102, 0\n"
          "v_mov_b32 v103, 0\n"
          "v_mov_b32 v104, 0\n"
          "v_mov_b32 v105, 0\n"
          "v_mov_b32 v106, 0\n"
          "v_mov_b32 v107, 0\n"
          "v_mov_b32 v108, 0\n"
          "v_mov_b32 v109, 0\n"
          "v_mov_b32 v110, 0\n"
          "v_mov_b32 v111, 0\n"
          "v_mov_b32 v112, 0\n"
          "v_mov_b32 v113, 0\n"
          "v_mov_b32 v114, 0\n"
          "v_mov_b32 v115, 0\n"
          "v_mov_b32 v116, 0\n"
          "v_mov_b32 v117, 0\n"
          "v_mov_b32 v118, 0\n"
          "v_mov_b32 v119, 0\n"
          "v_mov_b32 v120, 0\n"
          "v_mov_b32 v121, 0\n"
          "v_mov_b32 v122, 0\n"
          "v_mov_b32 v123, 0\n"
          "v_mov_b32 v124, 0\n"
          "v_mov_b32 v125, 0\n"
          "v_mov_b32 v126, 0\n"
          "v_mov_b32 v127, 0\n"
          "v_mov_b32 v128, 0\n"
          "v_mov_b32 v129, 0\n"
          "v_mov_b32 v130, 0\n"
          "v_mov_b32 v131, 0\n"
          "v_mov_b32 v132, 0\n"
          "v_mov_b32 v133, 0\n"
          "v_mov_b32 v134, 0\n"
          "v_mov_b32 v135, 0\n"
          "v_mov_b32 v136, 0\n"
          "v_mov_b32 v137, 0\n"
          "v_mov_b32 v138, 0\n"
          "v_mov_b32 v139, 0\n"
          "v_mov_b32 v140, 0\n"
          "v_mov_b32 v141, 0\n"
          "v_mov_b32 v142, 0\n"
          "v_mov_b32 v143, 0\n"
          "s_sub_u32 s72, s62, s61\n"
          "s_add_u32 s72, s72, 31\n"
          "s_lshr_b32 s72, s72, 5\n"
          "s_mov_b32 s73, s61\n"
          "s_mov_b32 s75, s63\n"
          "s_waitcnt lgkmcnt(0)\n"
          "v_readlane_b32 s74, v217, 0\n"
          "s_lshl_b32 s83, s61, 2\n"
          "s_add_u32 s83, s83, 0\n"
          "v_lshl_add_u32 v229, v208, 2, s83\n"
          "s_add_u32 s84, s61, 17408\n"
          "v_add_u32 v230, s84, v208\n"
          "v_lshrrev_b32 v95, 3, v208\n"
          "v_lshl_add_u32 v228, v95, 2, s83\n"
          "ds_read2_b32 v[210:211], v228 offset0:0 offset1:8\n"
          "ds_read2_b32 v[212:213], v228 offset0:16 offset1:24\n"
          "ds_read2st64_b32 v[218:219], v229 offset0:0 offset1:1\n"
          "ds_read_u8 v209, v230\n"
          "s_add_u32 m0, s79, 0\n"
          "s_waitcnt lgkmcnt(3)\n"
          "v_mad_u32_u16 v210, v210, s80, v226\n"
          "buffer_load_dwordx4 v210, s[64:67], 0 offen lds\n"
          "s_add_u32 m0, s79, 1024\n"
          "v_mad_u32_u16 v211, v211, s80, v227\n"
          "buffer_load_dwordx4 v211, s[64:67], 0 offen lds\n"
          "s_add_u32 m0, s79, 2048\n"
          "s_waitcnt lgkmcnt(2)\n"
          "v_mad_u32_u16 v212, v212, s80, v226\n"
          "buffer_load_dwordx4 v212, s[64:67], 0 offen lds\n"
          "s_add_u32 m0, s79, 3072\n"
          "v_mad_u32_u16 v213, v213, s80, v227\n"
          "buffer_load_dwordx4 v213, s[64:67], 0 offen lds\n"
          "s_waitcnt lgkmcnt(1)\n"
          "v_mad_u32_u16 v95, v218, 16, 0\n"
          "buffer_load_dwordx3 v[214:216], v95, s[68:71], 0 offen\n"
          "ds_read2_b32 v[210:211], v228 offset0:32 offset1:40\n"
          "ds_read2_b32 v[212:213], v228 offset0:48 offset1:56\n"
          "s_add_u32 m0, s79, 4096\n"
          "s_waitcnt lgkmcnt(1)\n"
          "v_mad_u32_u16 v210, v210, s80, v226\n"
          "buffer_load_dwordx4 v210, s[64:67], 0 offen lds\n"
          "s_add_u32 m0, s79, 5120\n"
          "v_mad_u32_u16 v211, v211, s80, v227\n"
          "buffer_load_dwordx4 v211, s[64:67], 0 offen lds\n"
          "s_add_u32 m0, s79, 6144\n"
          "s_waitcnt lgkmcnt(0)\n"
          "v_mad_u32_u16 v212, v212, s80, v226\n"
          "buffer_load_dwordx4 v212, s[64:67], 0 offen lds\n"
          "s_add_u32 m0, s79, 7168\n"
          "v_mad_u32_u16 v213, v213, s80, v227\n"
          "buffer_load_dwordx4 v213, s[64:67], 0 offen lds\n"
          "s_cmp_eq_u32 s72, 0\n"
          "s_cbranch_scc1 LN0_%=\n"
          "v_min_u32 v182, s89, v209\n"
          "v_lshl_add_u32 v184, v182, 4, s85\n"
          "ds_read_b128 v[176:179], v184\n"
          "s_waitcnt vmcnt(4) lgkmcnt(0)\n"
          "v_pk_add_f32 v[176:177], v[176:177], v[214:215] neg_lo:[0,1] neg_hi:[0,1]\n"
          "v_sub_f32 v178, v178, v216\n"
          "v_cvt_pk_bf16_f32 v180, 1.0, v176\n"
          "v_cvt_pk_bf16_f32 v181, v177, v178\n"
          "v_mad_u32_u16 v184, v219, 16, 0\n"
          "v_and_b32 v176, 0xffff0000, v180\n"
          "v_lshlrev_b32 v177, 16, v181\n"
          "v_and_b32 v178, 0xffff0000, v181\n"
          "buffer_load_dwordx3 v[214:216], v184, s[68:71], 0 offen\n"
          "v_mul_f32 v176, v176, v176\n"
          "v_fmac_f32 v176, v177, v177\n"
          "v_fmac_f32 v176, v178, v178\n"
          "v_add_u32 v229, 0x100, v229\n"
          "v_cvt_pk_bf16_f32 v176, v176, v176\n"
          "v_add_u32 v230, 64, v230\n"
          "v_bfi_b32 v183, s86, v176, v218\n"
          "ds_write_b128 v238, v[180:183]\n"
          "ds_read2_b32 v[210:211], v228 offset0:64 offset1:72\n"
          "ds_read2_b32 v[212:213], v228 offset0:80 offset1:88\n"
          "ds_read_b128 v[176:179], v222 offset:0\n"
          "ds_read_b128 v[180:183], v223 offset:0\n"
          "ds_read_b128 v[184:187], v224 offset:0\n"
          "ds_read_b128 v[188:191], v225 offset:0\n"
          "ds_read_b32 v208, v231 offset:8\n"
          "s_waitcnt lgkmcnt(0)\n"
          "v_readfirstlane_b32 s77, v208\n"
          "s_lshl_b32 s78, s77, 3\n"
          "s_nop 0\n"
          "v_subrev_u32 v200, s77, v208\n"
          "v_cmp_le_u32 vcc, 8, v200\n"
          "s_cbranch_vccnz LR1P_%=\n"
          "LB1P_%=:\n"
          "v_mad_u32_u24 v200, v200, v234, v232\n"
          "v_mad_u32_u24 v198, s78, v234, v233\n"
          "ds_read_b128 v[200:203], v200 offset:0\n"
          "ds_read_b64_tr_b16 v[192:193], v198 offset:0\n"
          "ds_read_b64_tr_b16 v[194:195], v198 offset:512\n"
          "ds_read_b64_tr_b16 v[196:197], v198 offset:64\n"
          "ds_read_b64_tr_b16 v[198:199], v198 offset:576\n"
          "v_mfma_f32_32x32x16_bf16 v[0:15], v[176:179], v[144:147], 0\n"
          "v_mfma_f32_32x32x16_bf16 v[16:31], v[176:179], v[160:163], 0\n"
          "v_mfma_f32_32x32x16_bf16 v[0:15], v[180:183], v[148:151], v[0:15]\n"
          "v_mfma_f32_32x32x16_bf16 v[16:31], v[180:183], v[164:167], v[16:31]\n"
          "v_mfma_f32_32x32x16_bf16 v[0:15], v[184:187], v[152:155], v[0:15]\n"
          "v_mfma_f32_32x32x16_bf16 v[16:31], v[184:187], v[168:171], v[16:31]\n"
          "v_mfma_f32_32x32x16_bf16 v[0:15], v[188:191], v[156:159], v[0:15]\n"
          "v_mfma_f32_32x32x16_bf16 v[16:31], v[188:191], v[172:175], v[16:31]\n"
          "s_add_u32 m0, s79, 0\n"
          "v_mad_u32_u16 v210, v210, s80, v226\n"
          "buffer_load_dwordx4 v210, s[64:67], 0 offen lds\n"
          "s_add_u32 m0, s79, 1024\n"
          "v_mad_u32_u16 v211, v211, s80, v227\n"
          "buffer_load_dwordx4 v211, s[64:67], 0 offen lds\n"
          "s_add_u32 m0, s79, 2048\n"
          "v_mad_u32_u16 v212, v212, s80, v226\n"
          "buffer_load_dwordx4 v212, s[64:67], 0 offen lds\n"
          "s_add_u32 m0, s79, 3072\n"
          "v_mad_u32_u16 v213, v213, s80, v227\n"
          "buffer_load_dwordx4 v213, s[64:67], 0 offen lds\n"
          "s_waitcnt lgkmcnt(2)\n"
          "v_mfma_f32_32x32x16_bf16 v[0:15], v[200:203], v[192:195], v[0:15]\n"
          "s_waitcnt lgkmcnt(0)\n"
          "v_mfma_f32_32x32x16_bf16 v[16:31], v[200:203], v[196:199], v[16:31]\n"
          "s_cmp_lg_u32 s81, 0\n"
          "s_cbranch_scc1 LR2P_%=\n"
          "LB2P_%=:\n"
          "v_add_u32 v228, 0x180, v228\n"
          "v_mov_b32 v192, 0\n"
          "v_mov_b32 v193, 0\n"
          "v_mov_b32 v194, 0\n"
          "v_mov_b32 v195, 0\n"
          "v_mov_b32 v196, 0\n"
          "v_mov_b32 v197, 0\n"
          "v_mov_b32 v198, 0\n"
          "v_mov_b32 v199, 0\n"
          "v_mov_b32 v204, 0\n"
          "v_mov_b32 v205, 0\n"
          "v_mov_b32 v206, 0xffffff\n"
          "s_mul_i32 s58, s72, 8\n"
          "s_lshr_b32 s58, s58, 4\n"
          "s_setprio 1\n"
          "s_cmp_lt_u32 %[w], 4\n"
          "s_cselect_b32 s58, s58, 0\n"
          "s_cbranch_scc0 LSQ_%=\n"
          "s_setprio 2\n"
          "LSQ_%=:\n"
          "LIE_%=:\n"
          "s_waitcnt vmcnt(5)\n"
          "ds_read_b128 v[176:179], v222 offset:4096\n"
          "ds_read_b128 v[180:183], v223 offset:4096\n"
          "ds_read_b128 v[184:187], v224 offset:4096\n"
          "ds_read_b128 v[188:191], v225 offset:4096\n"
          "ds_read_b32 v208, v231 offset:520\n"
          "ds_read2_b32 v[210:211], v228 offset0:0 offset1:8\n"
          "ds_read2_b32 v[212:213], v228 offset0:16 offset1:24\n"
          "ds_read2st64_b32 v[218:219], v229 offset0:0 offset1:1\n"
          "ds_read_u8 v209, v230\n"
          "s_waitcnt lgkmcnt(12)\n"
          "v_mfma_f32_32x32x16_bf16 v[96:111], v[128:131], v[192:195], v[96:111]\n"
          "v_exp_f32 v64, v0\n"
          "v_exp_f32 v65, v1\n"
          "v_exp_f32 v66, v2\n"
          "v_mfma_f32_32x32x16_bf16 v[112:127], v[136:139], v[192:195], v[112:127]\n"
          "v_exp_f32 v67, v3\n"
          "v_exp_f32 v68, v4\n"
          "v_exp_f32 v69, v5\n"
          "s_waitcnt lgkmcnt(10)\n"
          "v_mfma_f32_32x32x16_bf16 v[96:111], v[132:135], v[196:199], v[96:111]\n"
          "v_exp_f32 v70, v6\n"
          "v_exp_f32 v71, v7\n"
          "v_exp_f32 v72, v8\n"
          "v_mfma_f32_32x32x16_bf16 v[112:127], v[140:143], v[196:199], v[112:127]\n"
          "v_exp_f32 v73, v9\n"
          "v_exp_f32 v74, v10\n"
          "v_exp_f32 v75, v11\n"
          "s_cmp_le_i32 s74, s73\n"
          "s_cbranch_scc1 LFE_%=\n"
          "LBFE_%=:\n"
          "s_waitcnt lgkmcnt(4)\n"
          "v_readfirstlane_b32 s77, v208\n"
          "s_lshl_b32 s78, s77, 3\n"
          "s_add_u32 s73, s73, 32\n"
          "v_subrev_u32 v204, s77, v208\n"
          "v_cmp_le_u32 vcc, 8, v204\n"
          "s_cbranch_vccnz LR1E_%=\n"
          "LB1E_%=:\n"
          "v_mad_u32_u24 v204, v204, v234, v232\n"
          "v_mad_u32_u24 v198, s78, v234, v233\n"
          "ds_read_b128 v[204:207], v204 offset:512\n"
          "ds_read_b64_tr_b16 v[192:193], v198 offset:0\n"
          "ds_read_b64_tr_b16 v[194:195], v198 offset:512\n"
          "ds_read_b64_tr_b16 v[196:197], v198 offset:64\n"
          "ds_read_b64_tr_b16 v[198:199], v198 offset:576\n"
          "v_mfma_f32_32x32x16_bf16 v[32:47], v[176:179], v[144:147], 0\n"
          "v_exp_f32 v76, v12\n"
          "v_exp_f32 v77, v13\n"
          "v_exp_f32 v78, v14\n"
          "v_mfma_f32_32x32x16_bf16 v[48:63], v[176:179], v[160:163], 0\n"
          "v_exp_f32 v79, v15\n"
          "v_exp_f32 v80, v16\n"
          "v_exp_f32 v81, v17\n"
          "v_mfma_f32_32x32x16_bf16 v[32:47], v[180:183], v[148:151], v[32:47]\n"
          "v_exp_f32 v82, v18\n"
          "v_exp_f32 v83, v19\n"
          "v_exp_f32 v84, v20\n"
          "v_mfma_f32_32x32x16_bf16 v[48:63], v[180:183], v[164:167], v[48:63]\n"
          "v_exp_f32 v85, v21\n"
          "v_exp_f32 v86, v22\n"
          "v_exp_f32 v87, v23\n"
          "v_exp_f32 v88, v24\n"
          "v_exp_f32 v89, v25\n"
          "v_exp_f32 v90, v26\n"
          "v_exp_f32 v91, v27\n"
          "v_exp_f32 v92, v28\n"
          "v_exp_f32 v93, v29\n"
          "v_exp_f32 v94, v30\n"
          "v_exp_f32 v95, v31\n"
          "s_add_u32 m0, s79, 4096\n"
          "s_waitcnt lgkmcnt(8)\n"
          "v_mad_u32_u16 v210, v210, s80, v226\n"
          "buffer_load_dwordx4 v210, s[64:67], 0 offen lds\n"
          "s_add_u32 m0, s79, 5120\n"
          "v_mad_u32_u16 v211, v211, s80, v227\n"
          "buffer_load_dwordx4 v211, s[64:67], 0 offen lds\n"
          "s_add_u32 m0, s79, 6144\n"
          "s_waitcnt lgkmcnt(7)\n"
          "v_mad_u32_u16 v212, v212, s80, v226\n"
          "buffer_load_dwordx4 v212, s[64:67], 0 offen lds\n"
          "s_add_u32 m0, s79, 7168\n"
          "v_mad_u32_u16 v213, v213, s80, v227\n"
          "buffer_load_dwordx4 v213, s[64:67], 0 offen lds\n"
          "v_pk_add_f32 v[64:65], v[64:65], 1.0 op_sel_hi:[1,0]\n"
          "v_pk_add_f32 v[66:67], v[66:67], 1.0 op_sel_hi:[1,0]\n"
          "v_pk_add_f32 v[68:69], v[68:69], 1.0 op_sel_hi:[1,0]\n"
          "v_pk_add_f32 v[70:71], v[70:71], 1.0 op_sel_hi:[1,0]\n"
          "v_pk_add_f32 v[72:73], v[72:73], 1.0 op_sel_hi:[1,0]\n"
          "v_pk_add_f32 v[74:75], v[74:75], 1.0 op_sel_hi:[1,0]\n"
          "v_pk_add_f32 v[76:77], v[76:77], 1.0 op_sel_hi:[1,0]\n"
          "v_pk_add_f32 v[78:79], v[78:79], 1.0 op_sel_hi:[1,0]\n"
          "v_pk_add_f32 v[80:81], v[80:81], 1.0 op_sel_hi:[1,0]\n"
          "v_pk_add_f32 v[82:83], v[82:83], 1.0 op_sel_hi:[1,0]\n"
          "v_pk_add_f32 v[84:85], v[84:85], 1.0 op_sel_hi:[1,0]\n"
          "v_pk_add_f32 v[86:87], v[86:87], 1.0 op_sel_hi:[1,0]\n"
          "v_pk_add_f32 v[88:89], v[88:89], 1.0 op_sel_hi:[1,0]\n"
          "v_pk_add_f32 v[90:91], v[90:91], 1.0 op_sel_hi:[1,0]\n"
          "v_pk_add_f32 v[92:93], v[92:93], 1.0 op_sel_hi:[1,0]\n"
          "v_pk_add_f32 v[94:95], v[94:95], 1.0 op_sel_hi:[1,0]\n"
          "v_mfma_f32_32x32x16_bf16 v[32:47], v[184:187], v[152:155], v[32:47]\n"
          "v_rcp_f32 v64, v64\n"
          "v_rcp_f32 v65, v65\n"
          "v_rcp_f32 v66, v66\n"
          "v_mfma_f32_32x32x16_bf16 v[48:63], v[184:187], v[168:171], v[48:63]\n"
          "v_rcp_f32 v67, v67\n"
          "v_rcp_f32 v68, v68\n"
          "v_rcp_f32 v69, v69\n"
          "v_mfma_f32_32x32x16_bf16 v[32:47], v[188:191], v[156:159], v[32:47]\n"
          "v_rcp_f32 v70, v70\n"
          "v_rcp_f32 v71, v71\n"
          "v_rcp_f32 v72, v72\n"
          "v_mfma_f32_32x32x16_bf16 v[48:63], v[188:191], v[172:175], v[48:63]\n"
          "v_rcp_f32 v73, v73\n"
          "v_rcp_f32 v74, v74\n"
          "v_rcp_f32 v75, v75\n"
          "s_waitcnt lgkmcnt(2)\n"
          "v_mfma_f32_32x32x16_bf16 v[32:47], v[204:207], v[192:195], v[32:47]\n"
          "v_rcp_f32 v76, v76\n"
          "v_rcp_f32 v77, v77\n"
          "v_rcp_f32 v78, v78\n"
          "s_waitcnt lgkmcnt(0)\n"
          "v_mfma_f32_32x32x16_bf16 v[48:63], v[204:207], v[196:199], v[48:63]\n"
          "v_rcp_f32 v79, v79\n"
          "v_rcp_f32 v80, v80\n"
          "v_rcp_f32 v81, v81\n"
          "s_cmp_lg_u32 s81, 0\n"
          "s_cbranch_scc1 LR2E_%=\n"
          "LB2E_%=:\n"
          "v_subrev_u32 v208, s75, v202\n"
          "v_min_u32 v208, 8, v208\n"
          "v_mad_u32_u24 v208, v208, v235, v236\n"
          "ds_write_b64 v208, v[200:201]\n"
          "ds_read_b64_tr_b16 v[192:193], v237 offset:0\n"
          "ds_read_b64_tr_b16 v[194:195], v237 offset:576\n"
          "ds_read_b64_tr_b16 v[196:197], v237 offset:1152\n"
          "ds_read_b64_tr_b16 v[198:199], v237 offset:1728\n"
          "ds_write_b64 v208, v[220:221]\n"
          "v_min_u32 v182, s89, v209\n"
          "v_lshl_add_u32 v184, v182, 4, s85\n"
          "ds_read_b128 v[176:179], v184\n"
          "v_rcp_f32 v82, v82\n"
          "v_rcp_f32 v83, v83\n"
          "v_rcp_f32 v84, v84\n"
          "v_rcp_f32 v85, v85\n"
          "v_rcp_f32 v86, v86\n"
          "v_rcp_f32 v87, v87\n"
          "v_rcp_f32 v88, v88\n"
          "v_rcp_f32 v89, v89\n"
          "v_rcp_f32 v90, v90\n"
          "v_rcp_f32 v91, v91\n"
          "v_rcp_f32 v92, v92\n"
          "v_rcp_f32 v93, v93\n"
          "v_rcp_f32 v94, v94\n"
          "v_rcp_f32 v95, v95\n"
          "v_pk_mul_f32 v[64:65], v[64:65], v[0:1]\n"
          "v_pk_mul_f32 v[66:67], v[66:67], v[2:3]\n"
          "v_pk_mul_f32 v[68:69], v[68:69], v[4:5]\n"
          "v_pk_mul_f32 v[70:71], v[70:71], v[6:7]\n"
          "v_pk_mul_f32 v[72:73], v[72:73], v[8:9]\n"
          "v_pk_mul_f32 v[74:75], v[74:75], v[10:11]\n"
          "v_pk_mul_f32 v[76:77], v[76:77], v[12:13]\n"
          "v_pk_mul_f32 v[78:79], v[78:79], v[14:15]\n"
          "v_pk_mul_f32 v[80:81], v[80:81], v[16:17]\n"
          "v_pk_mul_f32 v[82:83], v[82:83], v[18:19]\n"
          "v_pk_mul_f32 v[84:85], v[84:85], v[20:21]\n"
          "v_pk_mul_f32 v[86:87], v[86:87], v[22:23]\n"
          "v_pk_mul_f32 v[88:89], v[88:89], v[24:25]\n"
          "v_pk_mul_f32 v[90:91], v[90:91], v[26:27]\n"
          "v_pk_mul_f32 v[92:93], v[92:93], v[28:29]\n"
          "v_pk_mul_f32 v[94:95], v[94:95], v[30:31]\n"
          "s_waitcnt vmcnt(8) lgkmcnt(0)\n"
          "v_pk_add_f32 v[176:177], v[176:177], v[214:215] neg_lo:[0,1] neg_hi:[0,1]\n"
          "v_sub_f32 v178, v178, v216\n"
          "v_cvt_pk_bf16_f32 v180, 1.0, v176\n"
          "v_cvt_pk_bf16_f32 v181, v177, v178\n"
          "v_mad_u32_u16 v184, v219, 16, 0\n"
          "v_and_b32 v176, 0xffff0000, v180\n"
          "v_lshlrev_b32 v177, 16, v181\n"
          "v_and_b32 v178, 0xffff0000, v181\n"
          "buffer_load_dwordx3 v[214:216], v184, s[68:71], 0 offen\n"
          "v_mul_f32 v176, v176, v176\n"
          "v_fmac_f32 v176, v177, v177\n"
          "v_fmac_f32 v176, v178, v178\n"
          "v_add_u32 v229, 0x100, v229\n"
          "v_cvt_pk_bf16_f32 v176, v176, v176\n"
          "v_add_u32 v230, 64, v230\n"
          "v_bfi_b32 v183, s86, v176, v218\n"
          "ds_write_b128 v238, v[180:183]\n"
          "v_cvt_pk_bf16_f32 v128, v64, v65\n"
          "v_cvt_pk_bf16_f32 v129, v66, v67\n"
          "v_cvt_pk_bf16_f32 v130, v68, v69\n"
          "v_cvt_pk_bf16_f32 v131, v70, v71\n"
          "v_cvt_pk_bf16_f32 v132, v72, v73\n"
          "v_cvt_pk_bf16_f32 v133, v74, v75\n"
          "v_cvt_pk_bf16_f32 v134, v76, v77\n"
          "v_cvt_pk_bf16_f32 v135, v78, v79\n"
          "v_cvt_pk_bf16_f32 v136, v80, v81\n"
          "v_cvt_pk_bf16_f32 v137, v82, v83\n"
          "v_cvt_pk_bf16_f32 v138, v84, v85\n"
          "v_cvt_pk_bf16_f32 v139, v86, v87\n"
          "v_cvt_pk_bf16_f32 v140, v88, v89\n"
          "v_cvt_pk_bf16_f32 v141, v90, v91\n"
          "v_cvt_pk_bf16_f32 v142, v92, v93\n"
          "v_cvt_pk_bf16_f32 v143, v94, v95\n"
          "s_sub_u32 s72, s72, 1\n"
          "s_cmp_eq_u32 s72, 0\n"
          "s_cbranch_scc1 LTE_%=\n"
          "LIO_%=:\n"
          "s_waitcnt vmcnt(5)\n"
          "ds_read_b128 v[176:179], v222 offset:0\n"
          "ds_read_b128 v[180:183], v223 offset:0\n"
          "ds_read_b128 v[184:187], v224 offset:0\n"
          "ds_read_b128 v[188:191], v225 offset:0\n"
          "ds_read_b32 v208, v231 offset:8\n"
          "ds_read2_b32 v[210:211], v228 offset0:32 offset1:40\n"
          "ds_read2_b32 v[212:213], v228 offset0:48 offset1:56\n"
          "v_mfma_f32_32x32x16_bf16 v[96:111], v[128:131], v[192:195], v[96:111]\n"
          "v_exp_f32 v64, v32\n"
          "v_exp_f32 v65, v33\n"
          "v_exp_f32 v66, v34\n"
          "v_mfma_f32_32x32x16_bf16 v[112:127], v[136:139], v[192:195], v[112:127]\n"
          "v_exp_f32 v67, v35\n"
          "v_exp_f32 v68, v36\n"
          "v_exp_f32 v69, v37\n"
          "v_mfma_f32_32x32x16_bf16 v[96:111], v[132:135], v[196:199], v[96:111]\n"
          "v_exp_f32 v70, v38\n"
          "v_exp_f32 v71, v39\n"
          "v_exp_f32 v72, v40\n"
          "v_mfma_f32_32x32x16_bf16 v[112:127], v[140:143], v[196:199], v[112:127]\n"
          "v_exp_f32 v73, v41\n"
          "v_exp_f32 v74, v42\n"
          "v_exp_f32 v75, v43\n"
          "s_cmp_le_i32 s74, s73\n"
          "s_cbranch_scc1 LFO_%=\n"
          "LBFO_%=:\n"
          "s_waitcnt lgkmcnt(2)\n"
          "v_readfirstlane_b32 s77, v208\n"
          "s_lshl_b32 s78, s77, 3\n"
          "s_add_u32 s73, s73, 32\n"
          "v_subrev_u32 v200, s77, v208\n"
          "v_cmp_le_u32 vcc, 8, v200\n"
          "s_cbranch_vccnz LR1O_%=\n"
          "LB1O_%=:\n"
          "v_mad_u32_u24 v200, v200, v234, v232\n"
          "v_mad_u32_u24 v198, s78, v234, v233\n"
          "ds_read_b128 v[200:203], v200 offset:0\n"
          "ds_read_b64_tr_b16 v[192:193], v198 offset:0\n"
          "ds_read_b64_tr_b16 v[194:195], v198 offset:512\n"
          "ds_read_b64_tr_b16 v[196:197], v198 offset:64\n"
          "ds_read_b64_tr_b16 v[198:199], v198 offset:576\n"
          "v_mfma_f32_32x32x16_bf16 v[0:15], v[176:179], v[144:147], 0\n"
          "v_exp_f32 v76, v44\n"
          "v_exp_f32 v77, v45\n"
          "v_exp_f32 v78, v46\n"
          "v_mfma_f32_32x32x16_bf16 v[16:31], v[176:179], v[160:163], 0\n"
          "v_exp_f32 v79, v47\n"
          "v_exp_f32 v80, v48\n"
          "v_exp_f32 v81, v49\n"
          "v_mfma_f32_32x32x16_bf16 v[0:15], v[180:183], v[148:151], v[0:15]\n"
          "v_exp_f32 v82, v50\n"
          "v_exp_f32 v83, v51\n"
          "v_exp_f32 v84, v52\n"
          "v_mfma_f32_32x32x16_bf16 v[16:31], v[180:183], v[164:167], v[16:31]\n"
          "v_exp_f32 v85, v53\n"
          "v_exp_f32 v86, v54\n"
          "v_exp_f32 v87, v55\n"
          "v_exp_f32 v88, v56\n"
          "v_exp_f32 v89, v57\n"
          "v_exp_f32 v90, v58\n"
          "v_exp_f32 v91, v59\n"
          "v_exp_f32 v92, v60\n"
          "v_exp_f32 v93, v61\n"
          "v_exp_f32 v94, v62\n"
          "v_exp_f32 v95, v63\n"
          "s_add_u32 m0, s79, 0\n"
          "s_waitcnt lgkmcnt(6)\n"
          "v_mad_u32_u16 v210, v210, s80, v226\n"
          "buffer_load_dwordx4 v210, s[64:67], 0 offen lds\n"
          "s_add_u32 m0, s79, 1024\n"
          "v_mad_u32_u16 v211, v211, s80, v227\n"
          "buffer_load_dwordx4 v211, s[64:67], 0 offen lds\n"
          "s_add_u32 m0, s79, 2048\n"
          "s_waitcnt lgkmcnt(5)\n"
          "v_mad_u32_u16 v212, v212, s80, v226\n"
          "buffer_load_dwordx4 v212, s[64:67], 0 offen lds\n"
          "s_add_u32 m0, s79, 3072\n"
          "v_mad_u32_u16 v213, v213, s80, v227\n"
          "buffer_load_dwordx4 v213, s[64:67], 0 offen lds\n"
          "v_add_u32 v228, 0x100, v228\n"
          "v_pk_add_f32 v[64:65], v[64:65], 1.0 op_sel_hi:[1,0]\n"
          "v_pk_add_f32 v[66:67], v[66:67], 1.0 op_sel_hi:[1,0]\n"
          "v_pk_add_f32 v[68:69], v[68:69], 1.0 op_sel_hi:[1,0]\n"
          "v_pk_add_f32 v[70:71], v[70:71], 1.0 op_sel_hi:[1,0]\n"
          "v_pk_add_f32 v[72:73], v[72:73], 1.0 op_sel_hi:[1,0]\n"
          "v_pk_add_f32 v[74:75], v[74:75], 1.0 op_sel_hi:[1,0]\n"
          "v_pk_add_f32 v[76:77], v[76:77], 1.0 op_sel_hi:[1,0]\n"
          "v_pk_add_f32 v[78:79], v[78:79], 1.0 op_sel_hi:[1,0]\n"
          "v_pk_add_f32 v[80:81], v[80:81], 1.0 op_sel_hi:[1,0]\n"
          "v_pk_add_f32 v[82:83], v[82:83], 1.0 op_sel_hi:[1,0]\n"
          "v_pk_add_f32 v[84:85], v[84:85], 1.0 op_sel_hi:[1,0]\n"
          "v_pk_add_f32 v[86:87], v[86:87], 1.0 op_sel_hi:[1,0]\n"
          "v_pk_add_f32 v[88:89], v[88:89], 1.0 op_sel_hi:[1,0]\n"
          "v_pk_add_f32 v[90:91], v[90:91], 1.0 op_sel_hi:[1,0]\n"
          "v_pk_add_f32 v[92:93], v[92:93], 1.0 op_sel_hi:[1,0]\n"
          "v_pk_add_f32 v[94:95], v[94:95], 1.0 op_sel_hi:[1,0]\n"
          "v_mfma_f32_32x32x16_bf16 v[0:15], v[184:187], v[152:155], v[0:15]\n"
          "v_rcp_f32 v64, v64\n"
          "v_rcp_f32 v65, v65\n"
          "v_rcp_f32 v66, v66\n"
          "v_mfma_f32_32x32x16_bf16 v[16:31], v[184:187], v[168:171], v[16:31]\n"
          "v_rcp_f32 v67, v67\n"
          "v_rcp_f32 v68, v68\n"
          "v_rcp_f32 v69, v69\n"
          "v_mfma_f32_32x32x16_bf16 v[0:15], v[188:191], v[156:159], v[0:15]\n"
          "v_rcp_f32 v70, v70\n"
          "v_rcp_f32 v71, v71\n"
          "v_rcp_f32 v72, v72\n"
          "v_mfma_f32_32x32x16_bf16 v[16:31], v[188:191], v[172:175], v[16:31]\n"
          "v_rcp_f32 v73, v73\n"
          "v_rcp_f32 v74, v74\n"
          "v_rcp_f32 v75, v75\n"
          "s_waitcnt lgkmcnt(2)\n"
          "v_mfma_f32_32x32x16_bf16 v[0:15], v[200:203], v[192:195], v[0:15]\n"
          "v_rcp_f32 v76, v76\n"
          "v_rcp_f32 v77, v77\n"
          "v_rcp_f32 v78, v78\n"
          "s_waitcnt lgkmcnt(0)\n"
          "v_mfma_f32_32x32x16_bf16 v[16:31], v[200:203], v[196:199], v[16:31]\n"
          "v_rcp_f32 v79, v79\n"
          "v_rcp_f32 v80, v80\n"
          "v_rcp_f32 v81, v81\n"
          "s_cmp_lg_u32 s81, 0\n"
          "s_cbranch_scc1 LR2O_%=\n"
          "LB2O_%=:\n"
          "v_subrev_u32 v208, s75, v206\n"
          "v_min_u32 v208, 8, v208\n"
          "v_mad_u32_u24 v208, v208, v235, v236\n"
          "ds_write_b64 v208, v[204:205]\n"
          "ds_read_b64_tr_b16 v[192:193], v237 offset:0\n"
          "ds_read_b64_tr_b16 v[194:195], v237 offset:576\n"
          "ds_read_b64_tr_b16 v[196:197], v237 offset:1152\n"
          "ds_read_b64_tr_b16 v[198:199], v237 offset:1728\n"
          "ds_write_b64 v208, v[220:221]\n"
          "v_rcp_f32 v82, v82\n"
          "v_rcp_f32 v83, v83\n"
          "v_rcp_f32 v84, v84\n"
          "v_rcp_f32 v85, v85\n"
          "v_rcp_f32 v86, v86\n"
          "v_rcp_f32 v87, v87\n"
          "v_rcp_f32 v88, v88\n"
          "v_rcp_f32 v89, v89\n"
          "v_rcp_f32 v90, v90\n"
          "v_rcp_f32 v91, v91\n"
          "v_rcp_f32 v92, v92\n"
          "v_rcp_f32 v93, v93\n"
          "v_rcp_f32 v94, v94\n"
          "v_rcp_f32 v95, v95\n"
          "v_pk_mul_f32 v[64:65], v[64:65], v[32:33]\n"
          "v_pk_mul_f32 v[66:67], v[66:67], v[34:35]\n"
          "v_pk_mul_f32 v[68:69], v[68:69], v[36:37]\n"
          "v_pk_mul_f32 v[70:71], v[70:71], v[38:39]\n"
          "v_pk_mul_f32 v[72:73], v[72:73], v[40:41]\n"
          "v_pk_mul_f32 v[74:75], v[74:75], v[42:43]\n"
          "v_pk_mul_f32 v[76:77], v[76:77], v[44:45]\n"
          "v_pk_mul_f32 v[78:79], v[78:79], v[46:47]\n"
          "v_pk_mul_f32 v[80:81], v[80:81], v[48:49]\n"
          "v_pk_mul_f32 v[82:83], v[82:83], v[50:51]\n"
          "v_pk_mul_f32 v[84:85], v[84:85], v[52:53]\n"
          "v_pk_mul_f32 v[86:87], v[86:87], v[54:55]\n"
          "v_pk_mul_f32 v[88:89], v[88:89], v[56:57]\n"
          "v_pk_mul_f32 v[90:91], v[90:91], v[58:59]\n"
          "v_pk_mul_f32 v[92:93], v[92:93], v[60:61]\n"
          "v_pk_mul_f32 v[94:95], v[94:95], v[62:63]\n"
          "v_cvt_pk_bf16_f32 v128, v64, v65\n"
          "v_cvt_pk_bf16_f32 v129, v66, v67\n"
          "v_cvt_pk_bf16_f32 v130, v68, v69\n"
          "v_cvt_pk_bf16_f32 v131, v70, v71\n"
          "v_cvt_pk_bf16_f32 v132, v72, v73\n"
          "v_cvt_pk_bf16_f32 v133, v74, v75\n"
          "v_cvt_pk_bf16_f32 v134, v76, v77\n"
          "v_cvt_pk_bf16_f32 v135, v78, v79\n"
          "v_cvt_pk_bf16_f32 v136, v80, v81\n"
          "v_cvt_pk_bf16_f32 v137, v82, v83\n"
          "v_cvt_pk_bf16_f32 v138, v84, v85\n"
          "v_cvt_pk_bf16_f32 v139, v86, v87\n"
          "v_cvt_pk_bf16_f32 v140, v88, v89\n"
          "v_cvt_pk_bf16_f32 v141, v90, v91\n"
          "v_cvt_pk_bf16_f32 v142, v92, v93\n"
          "v_cvt_pk_bf16_f32 v143, v94, v95\n"
          "s_cmp_le_u32 s72, s58\n"
          "s_cbranch_scc0 LSP_%=\n"
          "s_setprio 0\n"
          "LSP_%=:\n"
          "s_sub_u32 s72, s72, 1\n"
          "s_cmp_eq_u32 s72, 0\n"
          "s_cbranch_scc0 LIE_%=\n"
          "v_mov_b32 v200, v204\n"
          "v_mov_b32 v201, v205\n"
          "v_mov_b32 v202, v206\n"
          "LTE_%=:\n"
          "s_waitcnt lgkmcnt(3)\n"
          "v_mfma_f32_32x32x16_bf16 v[96:111], v[128:131], v[192:195], v[96:111]\n"
          "v_mfma_f32_32x32x16_bf16 v[112:127], v[136:139], v[192:195], v[112:127]\n"
          "s_waitcnt lgkmcnt(1)\n"
          "v_mfma_f32_32x32x16_bf16 v[96:111], v[132:135], v[196:199], v[96:111]\n"
          "v_mfma_f32_32x32x16_bf16 v[112:127], v[140:143], v[196:199], v[112:127]\n"
          "LFT_%=:\n"
          "v_lshl_add_u32 v192, v235, 1, s94\n"
          "ds_read_b128 v[16:19], v192 offset:0\n"
          "ds_read_b128 v[20:23], v192 offset:32\n"
          "ds_read_b128 v[24:27], v192 offset:64\n"
          "ds_read_b128 v[28:31], v192 offset:96\n"
          "s_sub_u32 s83, s89, s75\n"
          "s_min_u32 s83, s83, 8\n"
          "s_lshl_b32 s83, s83, 2\n"
          "s_bfm_b64 s[90:91], s83, 0\n"
          "s_lshl_b32 s84, s75, 7\n"
          "v_add_u32 v194, s84, v239\n"
          "s_lshl_b32 s84, s75, 4\n"
          "v_add_u32 v195, s84, v240\n"
          "v_cvt_pk_bf16_f32 v0, v96, v97\n"
          "v_cvt_pk_bf16_f32 v1, v98, v99\n"
          "v_cvt_pk_bf16_f32 v2, v100, v101\n"
          "v_cvt_pk_bf16_f32 v3, v102, v103\n"
          "v_cvt_pk_bf16_f32 v4, v104, v105\n"
          "v_cvt_pk_bf16_f32 v5, v106, v107\n"
          "v_cvt_pk_bf16_f32 v6, v108, v109\n"
          "v_cvt_pk_bf16_f32 v7, v110, v111\n"
          "v_cvt_pk_bf16_f32 v8, v112, v113\n"
          "v_cvt_pk_bf16_f32 v9, v114, v115\n"
          "v_cvt_pk_bf16_f32 v10, v116, v117\n"
          "v_cvt_pk_bf16_f32 v11, v118, v119\n"
          "v_cvt_pk_bf16_f32 v12, v120, v121\n"
          "v_cvt_pk_bf16_f32 v13, v122, v123\n"
          "v_cvt_pk_bf16_f32 v14, v124, v125\n"
          "v_cvt_pk_bf16_f32 v15, v126, v127\n"
          "s_and_b32 exec_lo, s90, 0x11111111\n"
          "s_and_b32 exec_hi, s90, 0x11111111\n"
          "ds_write_b128 v194, v[0:3] offset:0\n"
          "ds_write_b128 v194, v[4:7] offset:16\n"
          "ds_write_b128 v194, v[8:11] offset:64\n"
          "ds_write_b128 v194, v[12:15] offset:80\n"
          "s_mov_b64 exec, -1\n"
          "s_waitcnt lgkmcnt(7)\n"
          "v_mfma_f32_32x32x16_bf16 v[96:111], v[16:19], v[0:3], 0\n"
          "s_waitcnt lgkmcnt(6)\n"
          "v_mfma_f32_32x32x16_bf16 v[96:111], v[20:23], v[4:7], v[96:111]\n"
          "s_waitcnt lgkmcnt(5)\n"
          "v_mfma_f32_32x32x16_bf16 v[96:111], v[24:27], v[8:11], v[96:111]\n"
          "s_waitcnt lgkmcnt(4)\n"
          "v_mfma_f32_32x32x16_bf16 v[96:111], v[28:31], v[12:15], v[96:111]\n"
          "s_add_u32 s75, s75, 8\n"
          "s_add_u32 s76, s76, 1\n"
          "v_readlane_b32 s74, v217, s76\n"
          "v_mov_b32 v16, v195\n"
          "v_subrev_u32 v17, s75, v202\n"
          "v_min_u32 v17, 8, v17\n"
          "v_mad_u32_u24 v17, v17, v235, v236\n"
          "ds_write_b64 v17, v[200:201]\n"
          "ds_read_b64_tr_b16 v[192:193], v237 offset:0\n"
          "ds_read_b64_tr_b16 v[194:195], v237 offset:576\n"
          "ds_read_b64_tr_b16 v[196:197], v237 offset:1152\n"
          "ds_read_b64_tr_b16 v[198:199], v237 offset:1728\n"
          "ds_write_b64 v17, v[220:221]\n"
          "s_and_b32 exec_lo, s90, 0xeeeeeeee\n"
          "s_mov_b32 exec_hi, 0\n"
          "ds_write_b32 v16, v96\n"
          "s_mov_b64 exec, -1\n"
          "s_waitcnt lgkmcnt(4)\n"
          "v_mfma_f32_32x32x16_bf16 v[112:127], v[136:139], v[192:195], 0\n"
          "s_waitcnt lgkmcnt(2)\n"
          "v_mfma_f32_32x32x16_bf16 v[112:127], v[140:143], v[196:199], v[112:127]\n"
          "v_mfma_f32_32x32x16_bf16 v[96:111], v[128:131], v[192:195], 0\n"
          "v_mfma_f32_32x32x16_bf16 v[96:111], v[132:135], v[196:199], v[96:111]\n"
          "s_cmp_lt_u32 s75, s89\n"
          "s_cbranch_scc1 LFT_%=\n"
          "s_setprio 0\n"
          "s_waitcnt vmcnt(0) lgkmcnt(0)\n"
          "s_mov_b32 m0, s82\n"
          "s_mov_b32 s60, 0\n"
          "s_branch LEND_%=\n"
          "LN0_%=:\n"
          "v_mov_b32 v192, 0\n"
          "v_mov_b32 v193, 0\n"
          "v_mov_b32 v194, 0\n"
          "v_mov_b32 v195, 0\n"
          "v_mov_b32 v196, 0\n"
          "v_mov_b32 v197, 0\n"
          "v_mov_b32 v198, 0\n"
          "v_mov_b32 v199, 0\n"
          "v_mov_b32 v200, 0\n"
          "v_mov_b32 v201, 0\n"
          "v_mov_b32 v202, 0xffffff\n"
          "s_branch LTE_%=\n"
          "LFE_%=:\n"
          "v_lshl_add_u32 v192, v235, 1, s94\n"
          "ds_read_b128 v[48:51], v192 offset:0\n"
          "ds_read_b128 v[52:55], v192 offset:32\n"
          "ds_read_b128 v[56:59], v192 offset:64\n"
          "ds_read_b128 v[60:63], v192 offset:96\n"
          "s_sub_u32 s83, s89, s75\n"
          "s_min_u32 s83, s83, 8\n"
          "s_lshl_b32 s83, s83, 2\n"
          "s_bfm_b64 s[90:91], s83, 0\n"
          "s_lshl_b32 s84, s75, 7\n"
          "v_add_u32 v194, s84, v239\n"
          "s_lshl_b32 s84, s75, 4\n"
          "v_add_u32 v195, s84, v240\n"
          "v_cvt_pk_bf16_f32 v32, v96, v97\n"
          "v_cvt_pk_bf16_f32 v33, v98, v99\n"
          "v_cvt_pk_bf16_f32 v34, v100, v101\n"
          "v_cvt_pk_bf16_f32 v35, v102, v103\n"
          "v_cvt_pk_bf16_f32 v36, v104, v105\n"
          "v_cvt_pk_bf16_f32 v37, v106, v107\n"
          "v_cvt_pk_bf16_f32 v38, v108, v109\n"
          "v_cvt_pk_bf16_f32 v39, v110, v111\n"
          "v_cvt_pk_bf16_f32 v40, v112, v113\n"
          "v_cvt_pk_bf16_f32 v41, v114, v115\n"
          "v_cvt_pk_bf16_f32 v42, v116, v117\n"
          "v_cvt_pk_bf16_f32 v43, v118, v119\n"
          "v_cvt_pk_bf16_f32 v44, v120, v121\n"
          "v_cvt_pk_bf16_f32 v45, v122, v123\n"
          "v_cvt_pk_bf16_f32 v46, v124, v125\n"
          "v_cvt_pk_bf16_f32 v47, v126, v127\n"
          "s_and_b32 exec_lo, s90, 0x11111111\n"
          "s_and_b32 exec_hi, s90, 0x11111111\n"
          "ds_write_b128 v194, v[32:35] offset:0\n"
          "ds_write_b128 v194, v[36:39] offset:16\n"
          "ds_write_b128 v194, v[40:43] offset:64\n"
          "ds_write_b128 v194, v[44:47] offset:80\n"
          "s_mov_b64 exec, -1\n"
          "s_waitcnt lgkmcnt(7)\n"
          "v_mfma_f32_32x32x16_bf16 v[96:111], v[48:51], v[32:35], 0\n"
          "s_waitcnt lgkmcnt(6)\n"
          "v_mfma_f32_32x32x16_bf16 v[96:111], v[52:55], v[36:39], v[96:111]\n"
          "s_waitcnt lgkmcnt(5)\n"
          "v_mfma_f32_32x32x16_bf16 v[96:111], v[56:59], v[40:43], v[96:111]\n"
          "s_waitcnt lgkmcnt(4)\n"
          "v_mfma_f32_32x32x16_bf16 v[96:111], v[60:63], v[44:47], v[96:111]\n"
          "s_add_u32 s75, s75, 8\n"
          "s_add_u32 s76, s76, 1\n"
          "v_readlane_b32 s74, v217, s76\n"
          "v_mov_b32 v48, v195\n"
          "v_subrev_u32 v49, s75, v206\n"
          "v_min_u32 v49, 8, v49\n"
          "v_mad_u32_u24 v49, v49, v235, v236\n"
          "ds_write_b64 v49, v[204:205]\n"
          "ds_read_b64_tr_b16 v[192:193], v237 offset:0\n"
          "ds_read_b64_tr_b16 v[194:195], v237 offset:576\n"
          "ds_read_b64_tr_b16 v[196:197], v237 offset:1152\n"
          "ds_read_b64_tr_b16 v[198:199], v237 offset:1728\n"
          "ds_write_b64 v49, v[220:221]\n"
          "s_and_b32 exec_lo, s90, 0xeeeeeeee\n"
          "s_mov_b32 exec_hi, 0\n"
          "ds_write_b32 v48, v96\n"
          "s_mov_b64 exec, -1\n"
          "s_waitcnt lgkmcnt(4)\n"
          "v_mfma_f32_32x32x16_bf16 v[112:127], v[136:139], v[192:195], 0\n"
          "s_waitcnt lgkmcnt(2)\n"
          "v_mfma_f32_32x32x16_bf16 v[112:127], v[140:143], v[196:199], v[112:127]\n"
          "v_mfma_f32_32x32x16_bf16 v[96:111], v[128:131], v[192:195], 0\n"
          "v_mfma_f32_32x32x16_bf16 v[96:111], v[132:135], v[196:199], v[96:111]\n"
          "s_cmp_ge_u32 s75, s89\n"
          "s_cbranch_scc1 LBFE_%=\n"
          "s_cmp_le_i32 s74, s73\n"
          "s_cbranch_scc1 LFE_%=\n"
          "s_branch LBFE_%=\n"
          "LFO_%=:\n"
          "v_lshl_add_u32 v192, v235, 1, s94\n"
          "ds_read_b128 v[16:19], v192 offset:0\n"
          "ds_read_b128 v[20:23], v192 offset:32\n"
          "ds_read_b128 v[24:27], v192 offset:64\n"
          "ds_read_b128 v[28:31], v192 offset:96\n"
          "s_sub_u32 s83, s89, s75\n"
          "s_min_u32 s83, s83, 8\n"
          "s_lshl_b32 s83, s83, 2\n"
          "s_bfm_b64 s[90:91], s83, 0\n"
          "s_lshl_b32 s84, s75, 7\n"
          "v_add_u32 v194, s84, v239\n"
          "s_lshl_b32 s84, s75, 4\n"
          "v_add_u32 v195, s84, v240\n"
          "v_cvt_pk_bf16_f32 v0, v96, v97\n"
          "v_cvt_pk_bf16_f32 v1, v98, v99\n"
          "v_cvt_pk_bf16_f32 v2, v100, v101\n"
          "v_cvt_pk_bf16_f32 v3, v102, v103\n"
          "v_cvt_pk_bf16_f32 v4, v104, v105\n"
          "v_cvt_pk_bf16_f32 v5, v106, v107\n"
          "v_cvt_pk_bf16_f32 v6, v108, v109\n"
          "v_cvt_pk_bf16_f32 v7, v110, v111\n"
          "v_cvt_pk_bf16_f32 v8, v112, v113\n"
          "v_cvt_pk_bf16_f32 v9, v114, v115\n"
          "v_cvt_pk_bf16_f32 v10, v116, v117\n"
          "v_cvt_pk_bf16_f32 v11, v118, v119\n"
          "v_cvt_pk_bf16_f32 v12, v120, v121\n"
          "v_cvt_pk_bf16_f32 v13, v122, v123\n"
          "v_cvt_pk_bf16_f32 v14, v124, v125\n"
          "v_cvt_pk_bf16_f32 v15, v126, v127\n"
          "s_and_b32 exec_lo, s90, 0x11111111\n"
          "s_and_b32 exec_hi, s90, 0x11111111\n"
          "ds_write_b128 v194, v[0:3] offset:0\n"
          "ds_write_b128 v194, v[4:7] offset:16\n"
          "ds_write_b128 v194, v[8:11] offset:64\n"
          "ds_write_b128 v194, v[12:15] offset:80\n"
          "s_mov_b64 exec, -1\n"
          "s_waitcnt lgkmcnt(7)\n"
          "v_mfma_f32_32x32x16_bf16 v[96:111], v[16:19], v[0:3], 0\n"
          "s_waitcnt lgkmcnt(6)\n"
          "v_mfma_f32_32x32x16_bf16 v[96:111], v[20:23], v[4:7], v[96:111]\n"
          "s_waitcnt lgkmcnt(5)\n"
          "v_mfma_f32_32x32x16_bf16 v[96:111], v[24:27], v[8:11], v[96:111]\n"
          "s_waitcnt lgkmcnt(4)\n"
          "v_mfma_f32_32x32x16_bf16 v[96:111], v[28:31], v[12:15], v[96:111]\n"
          "s_add_u32 s75, s75, 8\n"
          "s_add_u32 s76, s76, 1\n"
          "v_readlane_b32 s74, v217, s76\n"
          "v_mov_b32 v16, v195\n"
          "v_subrev_u32 v17, s75, v202\n"
          "v_min_u32 v17, 8, v17\n"
          "v_mad_u32_u24 v17, v17, v235, v236\n"
          "ds_write_b64 v17, v[200:201]\n"
          "ds_read_b64_tr_b16 v[192:193], v237 offset:0\n"
          "ds_read_b64_tr_b16 v[194:195], v237 offset:576\n"
          "ds_read_b64_tr_b16 v[196:197], v237 offset:1152\n"
          "ds_read_b64_tr_b16 v[198:199], v237 offset:1728\n"
          "ds_write_b64 v17, v[220:221]\n"
          "s_and_b32 exec_lo, s90, 0xeeeeeeee\n"
          "s_mov_b32 exec_hi, 0\n"
          "ds_write_b32 v16, v96\n"
          "s_mov_b64 exec, -1\n"
          "s_waitcnt lgkmcnt(4)\n"
          "v_mfma_f32_32x32x16_bf16 v[112:127], v[136:139], v[192:195], 0\n"
          "s_waitcnt lgkmcnt(2)\n"
          "v_mfma_f32_32x32x16_bf16 v[112:127], v[140:143], v[196:199], v[112:127]\n"
          "v_mfma_f32_32x32x16_bf16 v[96:111], v[128:131], v[192:195], 0\n"
          "v_mfma_f32_32x32x16_bf16 v[96:111], v[132:135], v[196:199], v[96:111]\n"
          "s_cmp_ge_u32 s75, s89\n"
          "s_cbranch_scc1 LBFO_%=\n"
          "s_cmp_le_i32 s74, s73\n"
          "s_cbranch_scc1 LFO_%=\n"
          "s_branch LBFO_%=\n"
          "LR1P_%=:\n"
          "v_min_u32 v200, 8, v200\n"
          "s_mov_b32 s81, 1\n"
          "s_branch LB1P_%=\n"
          "LR2P_%=:\n"
          "s_mov_b32 s81, 0\n"
          "s_mov_b32 s88, s77\n"
          "v_lshlrev_b32 v188, 4, v235\n"
          "v_add_u32 v188, s87, v188\n"
          "LR2LP_%=:\n"
          "s_add_u32 s88, s88, 8\n"
          "s_lshl_b32 s83, s88, 3\n"
          "v_subrev_u32 v189, s88, v208\n"
          "v_cmp_le_i32 vcc, 8, v189\n"
          "v_min_u32 v189, 8, v189\n"
          "v_mad_u32_u24 v189, v189, v234, v188\n"
          "v_mad_u32_u24 v186, s83, v234, v233\n"
          "ds_read_b128 v[176:179], v189\n"
          "ds_read_b64_tr_b16 v[180:181], v186 offset:0\n"
          "ds_read_b64_tr_b16 v[182:183], v186 offset:512\n"
          "ds_read_b64_tr_b16 v[184:185], v186 offset:64\n"
          "ds_read_b64_tr_b16 v[186:187], v186 offset:576\n"
          "s_waitcnt lgkmcnt(2)\n"
          "v_mfma_f32_32x32x16_bf16 v[0:15], v[176:179], v[180:183], v[0:15]\n"
          "s_waitcnt lgkmcnt(0)\n"
          "v_mfma_f32_32x32x16_bf16 v[16:31], v[176:179], v[184:187], v[16:31]\n"
          "s_cbranch_vccz LB2P_%=\n"
          "s_cmp_lt_u32 s88, 200\n"
          "s_cbranch_scc1 LR2LP_%=\n"
          "s_branch LB2P_%=\n"
          "LR1E_%=:\n"
          "v_min_u32 v204, 8, v204\n"
          "s_mov_b32 s81, 1\n"
          "s_branch LB1E_%=\n"
          "LR2E_%=:\n"
          "s_mov_b32 s81, 0\n"
          "s_mov_b32 s88, s77\n"
          "v_lshlrev_b32 v188, 4, v235\n"
          "v_add_u32 v188, s87, v188\n"
          "LR2LE_%=:\n"
          "s_add_u32 s88, s88, 8\n"
          "s_lshl_b32 s83, s88, 3\n"
          "v_subrev_u32 v189, s88, v208\n"
          "v_cmp_le_i32 vcc, 8, v189\n"
          "v_min_u32 v189, 8, v189\n"
          "v_mad_u32_u24 v189, v189, v234, v188\n"
          "v_mad_u32_u24 v186, s83, v234, v233\n"
          "ds_read_b128 v[176:179], v189\n"
          "ds_read_b64_tr_b16 v[180:181], v186 offset:0\n"
          "ds_read_b64_tr_b16 v[182:183], v186 offset:512\n"
          "ds_read_b64_tr_b16 v[184:185], v186 offset:64\n"
          "ds_read_b64_tr_b16 v[186:187], v186 offset:576\n"
          "s_waitcnt lgkmcnt(2)\n"
          "v_mfma_f32_32x32x16_bf16 v[32:47], v[176:179], v[180:183], v[32:47]\n"
          "s_waitcnt lgkmcnt(0)\n"
          "v_mfma_f32_32x32x16_bf16 v[48:63], v[176:179], v[184:187], v[48:63]\n"
          "s_cbranch_vccz LB2E_%=\n"
          "s_cmp_lt_u32 s88, 200\n"
          "s_cbranch_scc1 LR2LE_%=\n"
          "s_branch LB2E_%=\n"
          "LR1O_%=:\n"
          "v_min_u32 v200, 8, v200\n"
          "s_mov_b32 s81, 1\n"
          "s_branch LB1O_%=\n"
          "LR2O_%=:\n"
          "s_mov_b32 s81, 0\n"
          "s_mov_b32 s88, s77\n"
          "v_lshlrev_b32 v188, 4, v235\n"
          "v_add_u32 v188, s87, v188\n"
          "LR2LO_%=:\n"
          "s_add_u32 s88, s88, 8\n"
          "s_lshl_b32 s83, s88, 3\n"
          "v_subrev_u32 v189, s88, v208\n"
          "v_cmp_le_i32 vcc, 8, v189\n"
          "v_min_u32 v189, 8, v189\n"
          "v_mad_u32_u24 v189, v189, v234, v188\n"
          "v_mad_u32_u24 v186, s83, v234, v233\n"
          "ds_read_b128 v[176:179], v189\n"
          "ds_read_b64_tr_b16 v[180:181], v186 offset:0\n"
          "ds_read_b64_tr_b16 v[182:183], v186 offset:512\n"
          "ds_read_b64_tr_b16 v[184:185], v186 offset:64\n"
          "ds_read_b64_tr_b16 v[186:187], v186 offset:576\n"
          "s_waitcnt lgkmcnt(2)\n"
          "v_mfma_f32_32x32x16_bf16 v[0:15], v[176:179], v[180:183], v[0:15]\n"
          "s_waitcnt lgkmcnt(0)\n"
          "v_mfma_f32_32x32x16_bf16 v[16:31], v[176:179], v[184:187], v[16:31]\n"
          "s_cbranch_vccz LB2O_%=\n"
          "s_cmp_lt_u32 s88, 200\n"
          "s_cbranch_scc1 LR2LO_%=\n"
          "s_branch LB2O_%=\n"
          "LEND_%=:\n"
          :
          : [e0] "s"(e0), [e1] "s"(e1), [nlo] "s"(nlo), [nhi] "s"(nhi), [w] "s"(w), [hlo] "s"((u32)hp), [hhi] "s"((u32)(hp >> 32)), [wlo] "s"((u32)wp), [whi] "s"((u32)(wp >> 32))
          : "v0", "v1", "v2", "v3", "v4", "v5", "v6", "v7", "v8", "v9", "v10", "v11", "v12", "v13", "v14", "v15", "v16", "v17", "v18", "v19", "v20", "v21", "v22", "v23", "v24", "v25", "v26", "v27", "v28", "v29", "v30", "v31", "v32", "v33", "v34", "v35", "v36", "v37", "v38", "v39", "v40", "v41", "v42", "v43", "v44", "v45", "v46", "v47", "v48", "v49", "v50", "v51", "v52", "v53", "v54", "v55", "v56", "v57", "v58", "v59", "v60", "v61", "v62", "v63", "v64", "v65", "v66", "v67", "v68", "v69", "v70", "v71", "v72", "v73", "v74", "v75", "v76", "v77", "v78", "v79", "v80", "v81", "v82", "v83", "v84", "v85", "v86", "v87", "v88", "v89", "v90", "v91", "v92", "v93", "v94", "v95", "v96", "v97", "v98", "v99", "v100", "v101", "v102", "v103", "v104", "v105", "v106", "v107", "v108", "v109", "v110", "v111", "v112", "v113", "v114", "v115", "v116", "v117", "v118", "v119", "v120", "v121", "v122", "v123", "v124", "v125", "v126", "v127", "v128", "v129", "v130", "v131", "v132", "v133", "v134", "v135", "v136", "v137", "v138", "v139", "v140", "v141", "v142", "v143", "v144", "v145", "v146", "v147", "v148", "v149", "v150", "v151", "v152", "v153", "v154", "v155", "v156", "v157", "v158", "v159", "v160", "v161", "v162", "v163", "v164", "v165", "v166", "v167", "v168", "v169", "v170", "v171", "v172", "v173", "v174", "v175", "v176", "v177", "v178", "v179", "v180", "v181", "v182", "v183", "v184", "v185", "v186", "v187", "v188", "v189", "v190", "v191", "v192", "v193", "v194", "v195", "v196", "v197", "v198", "v199", "v200", "v201", "v202", "v203", "v204", "v205", "v206", "v207", "v208", "v209", "v210", "v211", "v212", "v213", "v214", "v215", "v216", "v217", "v218", "v219", "v220", "v221", "v222", "v223", "v224", "v225", "v226", "v227", "v228", "v229", "v230", "v231", "v232", "v233", "v234", "v235", "v236", "v237", "v238", "v239", "v240", "v241", "v242", "s52", "s53", "s54", "s55", "s56", "s57", "s58", "s59", "s60", "s61", "s62", "s63", "s64", "s65", "s66", "s67", "s68", "s69", "s70", "s71", "s72", "s73", "s74", "s75", "s76", "s77", "s78", "s79", "s80", "s81", "s82", "s83", "s84", "s85", "s86", "s87", "s88", "s89", "s90", "s91", "s92", "s93", "s94", "s95", "vcc", "scc", "memory");
      }

    __syncthreads();

  } else {
    __syncthreads();
    float* acc = (float*)(smem + L_STG);
    u16* wl = (u16*)(smem + L_SP);
    float* pacc = (float*)(smem + L_PD);
    for (int i = tid; i < R * 64; i += 512) acc[i] = 0.f;
    for (int i = tid; i < 130 * 32; i += 512) ((u32*)wl)[i] = ((const u32*)we)[i];
    __syncthreads();
    for (int p = w; p < NP; p += 8) {
      const u32 o = ((const u32*)(ws + WS_OFF))[b * 256 + (p & 7) * 32 + (p >> 3)];
      const u32 st = o & 0xffffu, cnt = o >> 16;
      for (u32 j = 0; j < cnt; j++) {
        const u64 v = rec[p * CH + st + j];
        const u32 a = (u32)v;
        const int dl = (int)(v >> 34), sn = a & 0xffff;
        const f32x4 po = ((const f32x4*)(smem + L_PO))[dl];
        const f32x4 ps = posf[sn];
        const float dx = (float)(__bf16)(po.x - ps.x), dy = (float)(__bf16)(po.y - ps.y), dz = (float)(__bf16)(po.z - ps.z);
        const float d2 = (float)(__bf16)(dx * dx + dy * dy + dz * dz);
        float m, cs;
        slow_edge(h, wl, wx, lane, sn, n0 + dl, d2, bf2f(a >> 16), m, cs);
        atomicAdd(&acc[dl * 64 + lane], m);
        if (lane >= 1 && lane < 4) atomicAdd(&pacc[dl * 4 + lane], -LOG2E * cs * (lane == 1 ? dx : lane == 2 ? dy : dz));
      }
    }
    __syncthreads();
    for (int i = tid; i < R * 64; i += 512) ((u16*)(smem + L_AN))[i] = (u16)(pack2(-LOG2E * acc[i], 0.f) & 0xffff);
    __syncthreads();
  }

  if (w < 7) {
    const int row = 32 * w + r;
    u32x4 bh[4], bm[4];
#pragma unroll
    for (int s = 0; s < 4; s++)
      bh[s] = __builtin_bit_cast(u32x4, __builtin_amdgcn_raw_buffer_load_b128(srdH, (n0 + row) * 128 + (2 * s + hh) * 16, 0, 0));
#pragma unroll
    for (int s = 0; s < 4; s++) {
      bm[s] = *(const u32x4*)(smem + L_AN + row * 128 + (2 * s + hh) * 16);
    }
    f32x16 a0 = {}, a1 = {};
    const u32x4* whf = (const u32x4*)(ws + WS_WHF);
#pragma unroll
    for (int s = 0; s < 8; s++) {
      const bf16x8 bb = __builtin_bit_cast(bf16x8, s < 4 ? bh[s] : bm[s - 4]);
      a0 = __builtin_amdgcn_mfma_f32_32x32x16_bf16(__builtin_bit_cast(bf16x8, whf[s * 64 + lane]), bb, a0, 0, 0, 0);
      a1 = __builtin_amdgcn_mfma_f32_32x32x16_bf16(__builtin_bit_cast(bf16x8, whf[(8 + s) * 64 + lane]), bb, a1, 0, 0, 0);
    }
    if (row < Rb) {
#pragma unroll
      for (int s = 0; s < 4; s++) {
        u32x4 o;
#pragma unroll
        for (int i = 0; i < 4; i++) {
          const float y0 = s < 2 ? a0[8 * s + 2 * i] : a1[8 * (s - 2) + 2 * i];
          const float y1 = s < 2 ? a0[8 * s + 2 * i + 1] : a1[8 * (s - 2) + 2 * i + 1];
          const float g0 = y0 * __builtin_amdgcn_rcpf(1.f + __builtin_amdgcn_exp2f(y0));
          const float g1 = y1 * __builtin_amdgcn_rcpf(1.f + __builtin_amdgcn_exp2f(y1));
          o[i] = pack2(fmaf(g0, -LN2, bf2f(bh[s][i] & 0xffffu)), fmaf(g1, -LN2, __builtin_bit_cast(float, bh[s][i] & 0xffff0000u)));
        }
        *(u32x4*)(out + (size_t)(n0 + row) * 64 + (2 * s + hh) * 8) = o;
      }
    }
  }
  for (int i = tid; i < 3 * Rb; i += 512) {
    const int n = i / 3, c = i - 3 * n;
    const float v = ((const float*)(smem + L_PO))[n * 4 + c] - LN2 * ((const float*)(smem + L_PD))[n * 4 + 1 + c];
    out[(size_t)NN * 64 + (size_t)n0 * 3 + i] = (u16)(pack2(v, 0.f) & 0xffff);
  }
  }
}

}

extern "C" void kernel_launch(void* const* d_in, const int* in_sizes, int n_in, void* d_out, int out_size, void* d_ws,
                              size_t ws_size, hipStream_t stream) {
  const u16* h = (const u16*)d_in[0];
  const u16* pos = (const u16*)d_in[1];
  const u16* ea = (const u16*)d_in[2];
  const int* src = (const int*)d_in[3];
  const int* dst = (const int*)d_in[4];
  const u16* we = (const u16*)d_in[5];
  const u16* wx = (const u16*)d_in[6];
  const u16* wh = (const u16*)d_in[7];
  egnn_k1<<<NP, 1024, 0, stream>>>(src, dst, ea, pos, we, wx, wh, (char*)d_ws);
  egnn_k2<<<NB, 512, 0, stream>>>(h, we, wx, (const char*)d_ws, (u16*)d_out);
}
```
